```python
import torch

def _softmax(x, axis=-1):
    return torch.softmax(x, dim=axis)
def _gelu(x):
    return torch.nn.functional.gelu(x, approximate="tanh")
def _silu(x):
    return torch.nn.functional.silu(x)
def _sigmoid(x):
    return torch.sigmoid(x)
def _layernorm(x, axis=-1, eps=1e-5):
    m = x.mean(dim=axis, keepdim=True)
    v = x.var(dim=axis, keepdim=True, unbiased=False)
    return (x - m) / torch.sqrt(v + eps)
def _scatter_add(tgt, idx, src, axis=0):
    return tgt.clone().index_add_(axis, idx, src)
def _segment_sum(src, seg, n):
    out = torch.zeros((n,) + src.shape[1:], dtype=src.dtype)
    return out.index_add_(0, seg, src)
def _pad(x, pw):
    flat = tuple(v for pair in reversed(pw) for v in pair)
    return torch.nn.functional.pad(x, flat)

kernel_name = 'sequential_preLN__mha_block__mixer_token_mlp__prompt'

def setup_inputs():

    _w = (lambda a: (lambda t: t.to(torch.bfloat16) if t.is_floating_point() else t)(torch.as_tensor(a)))
    def _A_setup():
        B, S, D, H = 1, 2048, 576, 32
        return {
            "x": _w((lambda *s: torch.randn(*s, dtype=torch.bfloat16) * 0.05)(B, S, D)),
            "wqkv": _w((lambda *s: torch.randn(*s, dtype=torch.bfloat16) * 0.05)(D, 3 * D)),
            "wo": _w((lambda *s: torch.randn(*s, dtype=torch.bfloat16) * 0.05)(D, D)),
        }
    _A_in = _A_setup()

    def _B_setup():
        B, S, D, F = 1, 2048, 576, 4608
        return {
            "x": _w((lambda *s: torch.randn(*s, dtype=torch.bfloat16) * 0.05)(B, S, D)),
            "w1": _w((lambda *s: torch.randn(*s, dtype=torch.bfloat16) * 0.8)(S, F) * (S ** -0.5)),
            "b1": _w((lambda *s: torch.randn(*s, dtype=torch.bfloat16) * 12.8)(F) * 0.02),
            "w2": _w((lambda *s: torch.randn(*s, dtype=torch.bfloat16) * 0.4)(F, S) * (F ** -0.5)),
            "b2": _w((lambda *s: torch.randn(*s, dtype=torch.bfloat16) * 6.4)(S) * 0.02),
        }
    _B_in = _B_setup()

    return {
        "A_x": _A_in["x"],
        "A_wqkv": _A_in["wqkv"],
        "A_wo": _A_in["wo"],
        "B_x": _B_in["x"],
        "B_w1": _B_in["w1"],
        "B_b1": _B_in["b1"],
        "B_w2": _B_in["w2"],
        "B_b2": _B_in["b2"],
    }

def reference(A_x, A_wqkv, A_wo, B_x, B_w1, B_b1, B_w2, B_b2):
    def _A_ref(x, wqkv, wo):
        B, S, D = x.shape
        H = 32
        Dh = D // H
        qkv = (lambda x, *d: x.permute(*d))((x @ wqkv).reshape(B, S, 3, H, Dh), 2, 0, 3, 1, 4)
        q, k, v = qkv[0], qkv[1], qkv[2]
        scores = torch.einsum("bhqd,bhkd->bhqk", q, k) * (Dh ** -0.5)
        p = _softmax(scores, axis=-1)
        o = torch.einsum("bhqk,bhkd->bhqd", p, v)
        return (lambda x, *d: x.permute(*d))(o, 0, 2, 1, 3).reshape(B, S, D) @ wo

    def _B_ref(x, w1, b1, w2, b2):
        B, S, D = x.shape
        h = _layernorm(x, axis=-1)
        u = torch.einsum("bsd,sf->bdf", h, w1) + b1
        u = _gelu(u)
        y = torch.einsum("bdf,fs->bds", u, w2) + b2
        return (lambda x, *d: x.permute(*d))(y, 0, 2, 1)

    x = A_x
    h = x + _A_ref(_layernorm(x, axis=-1), A_wqkv, A_wo)
    h2 = h + _B_ref(_layernorm(h, axis=-1), B_w1, B_b1, B_w2, B_b2)
    return h2
```

```cpp
#include <hip/hip_runtime.h>
#include <hip/hip_bf16.h>
#include <stdint.h>

typedef short bf16x8 __attribute__((ext_vector_type(8)));
typedef short s16x4 __attribute__((ext_vector_type(4)));
typedef float f32x2 __attribute__((ext_vector_type(2)));
typedef float f32x4 __attribute__((ext_vector_type(4)));
typedef float f32x16 __attribute__((ext_vector_type(16)));
typedef unsigned u32x2 __attribute__((ext_vector_type(2)));
typedef unsigned u32x4 __attribute__((ext_vector_type(4)));
typedef __bf16 bf2_t __attribute__((ext_vector_type(2)));
#define LAS __attribute__((address_space(3)))
#define DI __device__ __forceinline__
typedef LAS char* lptr;

constexpr int S = 2048, D = 576, H = 32, F = 4608;
constexpr float LN_EPS = 1e-5f;
constexpr float QSCALE = 0.23570226039551584f * 1.4426950408889634f;
constexpr unsigned SENT = 0xFFFFFFFFu;

constexpr int QT = 1152, KVT = 2368;
constexpr size_t WS_QI  = 0;
constexpr size_t WS_KV  = WS_QI + (size_t)H * 64 * QT;
constexpr size_t WS_OI  = WS_KV + (size_t)H * 64 * KVT;
constexpr size_t WS_WO  = WS_OI + (size_t)128 * 18 * 1024;
constexpr size_t WS_HN  = WS_WO + (size_t)36 * 18 * 1024;
constexpr size_t WS_HR  = WS_HN + (size_t)36 * 64 * 1024;
constexpr size_t WS_UI  = WS_HR + (size_t)S * D * 2;
constexpr size_t WS_ST  = WS_UI + (size_t)36 * 144 * 1024;
constexpr size_t WS_E1  = WS_ST + 65536;
constexpr size_t E1_BYTES = (size_t)108 * 2 * 4 * 6 * 1024;
constexpr size_t WS_E2  = WS_E1 + E1_BYTES;
constexpr size_t E2_BYTES = (size_t)48 * 8 * 4 * 4 * 1536;
constexpr size_t WS_END = WS_E2 + E2_BYTES;

#define MF16(a, b, c) __builtin_amdgcn_mfma_f32_16x16x32_bf16((a), (b), (c), 0, 0, 0)
#define MF32(a, b, c) __builtin_amdgcn_mfma_f32_32x32x16_bf16((a), (b), (c), 0, 0, 0)

DI unsigned rfl(unsigned x) { return __builtin_amdgcn_readfirstlane(x); }
DI unsigned pk2(float lo, float hi) { f32x2 v = {lo, hi}; bf2_t b = __builtin_convertvector(v, bf2_t); return __builtin_bit_cast(unsigned, b); }
DI float bflo(unsigned u) { return __uint_as_float(u << 16); }
DI float bfhi(unsigned u) { return __uint_as_float(u & 0xFFFF0000u); }
DI unsigned nosent(unsigned u) { return u == SENT ? 0x7FC07FC0u : u; }
DI void glds(const void* base, unsigned voff, unsigned lds) {
    asm volatile("s_mov_b32 m0, %2\n\ts_nop 0\n\tglobal_load_lds_dwordx4 %0, %1" ::"v"(voff), "s"(base), "s"(lds) : "memory");
}
DI void glds_lo(const void* base, unsigned voff, unsigned lds) {
    asm volatile("s_mov_b32 m0, %2\n\ts_mov_b32 exec_hi, 0\n\tglobal_load_lds_dwordx4 %0, %1\n\ts_mov_b32 exec_hi, -1" ::"v"(voff), "s"(base), "s"(lds) : "memory");
}
template <int N> DI void wait_bar() { asm volatile("s_waitcnt vmcnt(%0) lgkmcnt(0)\n\ts_barrier" ::"i"(N) : "memory"); }
DI bf16x8 lds128(lptr p) { return *(const LAS bf16x8*)p; }
DI s16x4 ldstr(lptr p) { return __builtin_amdgcn_ds_read_tr16_b64_v4i16((LAS s16x4*)p); }
DI bf16x8 cat(s16x4 a, s16x4 b) { return __builtin_shufflevector(a, b, 0, 1, 2, 3, 4, 5, 6, 7); }
DI void st128_wt(void* base, unsigned voff, u32x4 v) { asm volatile("global_store_dwordx4 %0, %1, %2 sc1\n\ts_nop 1" ::"v"(voff), "v"(v), "s"(base) : "memory"); }
DI void st64_wt(void* base, unsigned voff, u32x2 v) { asm volatile("global_store_dwordx2 %0, %1, %2 sc1" ::"v"(voff), "v"(v), "s"(base) : "memory"); }
constexpr int SPIN_CAP = 1 << 15;

constexpr int K1_SLOT = 16384 + 18432, K1_TAB = 4 * K1_SLOT, K1_LDS = K1_TAB + 576 + 1024;

template <bool VOR>
DI void k1_body(const char* x, const char* w, char* ws, lptr lds, int rt, int cg) {
    const int tid = threadIdx.x, lane = tid & 63, wv = rfl(tid >> 6);
    const int n = lane & 15, g = lane >> 4, q = n >> 2, p = lane & 3;
    const unsigned l0 = (unsigned)(size_t)lds;
    unsigned xo[4], wo[5];
#pragma unroll
    for (int i = 0; i < 4; ++i) { const int row = 8 * i + (lane >> 3); xo[i] = (unsigned)((rt * 128 + 32 * wv + row) * 1152 + (((lane & 7) ^ ((row >> 1) & 7)) << 4)); }
#pragma unroll
    for (int i = 0; i < 5; ++i) { int idx = i * 64 + lane; if (idx > 287) idx = 287; const int pos = idx / 18, ch = idx - pos * 18;
        const int src = (pos & 3) | ((pos & 4) << 1) | ((pos & 8) >> 1); wo[i] = (unsigned)((16 * wv + src) * 3456 + cg * 288 + ch * 16); }
    auto stage = [&](int kb) {
        const unsigned sl = l0 + (kb & 3) * K1_SLOT; const char* xb = x + kb * 128; const char* wb = w + (size_t)kb * 64 * 3456;
#pragma unroll
        for (int i = 0; i < 4; ++i) glds(xb, xo[i], sl + wv * 4096 + i * 1024);
#pragma unroll
        for (int i = 0; i < 4; ++i) glds(wb, wo[i], sl + 16384 + wv * 4608 + i * 1024);
        glds_lo(wb, wo[4], sl + 16384 + wv * 4608 + 4096);
    };
    stage(0); stage(1); stage(2); stage(3);
    int wa[9];
    const int rowoff = (16 * (g >> 1) + 4 * (g & 1) + q) * 288 + 16384;
#pragma unroll
    for (int lt = 0; lt < 8; ++lt) { const int pp = ((lt >> 1) + wv) & 3;
        wa[lt] = rowoff + (VOR ? 32 * (2 * pp + (lt & 1)) + 8 * p : 72 * pp + 8 * (lt & 1) + (p & 1) * 16 + (p >> 1) * 40); }
    wa[8] = rowoff + (VOR ? 256 + 8 * p : 72 * p + 32);
    const int xa = wv * 4096 + n * 128;
    f32x4 acc[9][2], cw[3], sm[2], sq[2];
#pragma unroll
    for (int i = 0; i < 9; ++i) { acc[i][0] = f32x4{}; acc[i][1] = f32x4{}; }
    cw[0] = cw[1] = cw[2] = sm[0] = sm[1] = sq[0] = sq[1] = f32x4{};
    const bf16x8 ones = {0x3F80, 0x3F80, 0x3F80, 0x3F80, 0x3F80, 0x3F80, 0x3F80, 0x3F80};
    auto compute = [&](int kb) {
        const lptr sl = lds + (kb & 3) * K1_SLOT;
#pragma unroll
        for (int kk = 0; kk < 2; ++kk) {
            bf16x8 xf[2];
#pragma unroll
            for (int T = 0; T < 2; ++T) xf[T] = lds128(sl + xa + T * 2048 + (((4 * kk + g) ^ (n >> 1)) << 4));
#pragma unroll
            for (int T = 0; T < 2; ++T) { sm[T] = MF16(ones, xf[T], sm[T]); sq[T] = MF16(xf[T], xf[T], sq[T]); }
#pragma unroll
            for (int lt = 0; lt < 9; ++lt) {
                const bf16x8 wf = cat(ldstr(sl + wa[lt] + kk * 9216), ldstr(sl + wa[lt] + kk * 9216 + 2304));
#pragma unroll
                for (int T = 0; T < 2; ++T) acc[lt][T] = VOR ? MF16(xf[T], wf, acc[lt][T]) : MF16(wf, xf[T], acc[lt][T]);
                if (lt < 2) cw[lt] = MF16(wf, ones, cw[lt]);
                if (lt == 8) cw[2] = MF16(wf, ones, cw[2]);
            }
        }
    };
    wait_bar<27>(); compute(0);
    wait_bar<18>(); stage(4); compute(1);
    wait_bar<18>(); stage(5); compute(2);
    wait_bar<18>(); stage(6); compute(3);
    wait_bar<18>(); stage(7); compute(4);
    wait_bar<18>(); stage(8); compute(5);
    wait_bar<18>(); compute(6);
    wait_bar<9>(); compute(7);
    wait_bar<0>(); compute(8);
    LAS float* cwt = (LAS float*)(lds + K1_TAB); LAS f32x2* stt = (LAS f32x2*)(lds + K1_TAB + 576) + wv * 32;
    if (n == 0) { *(LAS f32x4*)(cwt + (2 * wv) * 16 + 4 * g) = cw[0]; *(LAS f32x4*)(cwt + (2 * wv + 1) * 16 + 4 * g) = cw[1]; if (wv == 0) *(LAS f32x4*)(cwt + 128 + 4 * g) = cw[2]; }
    if (q == g) {
#pragma unroll
        for (int T = 0; T < 2; ++T) { const float ss = p == 0 ? sq[T][0] : p == 1 ? sq[T][1] : p == 2 ? sq[T][2] : sq[T][3];
            const float mean = sm[T][0] * (1.f / D); const float var = fmaxf(ss * (1.f / D) - mean * mean, 0.f); stt[16 * T + n] = f32x2{mean, rsqrtf(var + LN_EPS)}; }
    }
    __syncthreads();
    const int sec = cg >> 2, hb = (cg & 3) * 8, tile32 = rt * 4 + wv;
    if (VOR) {
        f32x2 st[2][4];
#pragma unroll
        for (int T = 0; T < 2; ++T)
#pragma unroll
            for (int i = 0; i < 4; ++i) st[T][i] = stt[16 * T + 4 * g + i];
#pragma unroll
        for (int lt = 0; lt < 9; ++lt) { const int c = lt < 8 ? 2 * (((lt >> 1) + wv) & 3) + (lt & 1) : 8; const float c1 = cwt[c * 16 + n];
            float v[8];
#pragma unroll
            for (int T = 0; T < 2; ++T)
#pragma unroll
                for (int i = 0; i < 4; ++i) v[4 * T + i] = st[T][i][1] * (acc[lt][T][i] - st[T][i][0] * c1);
            const int col = 16 * c + n, hd = col / 18, dim = col - hd * 18;
            char* dst = ws + WS_KV + (size_t)((hb + hd) * 64 + tile32) * KVT + 1152 + (g >> 1) * 608 + (g & 1) * 304 + dim * 16;
            *(u32x4*)dst = u32x4{pk2(v[0], v[1]), pk2(v[2], v[3]), pk2(v[4], v[5]), pk2(v[6], v[7])};
            if (dim == 17) *(u32x4*)(dst + 16) = u32x4{0x3F803F80u, 0x3F803F80u, 0x3F803F80u, 0x3F803F80u};
        }
    } else {
        char* img = ws + (sec == 0 ? WS_QI : WS_KV); const int tb = sec == 0 ? QT : KVT; const float sc = sec == 0 ? QSCALE : 1.f;
#pragma unroll
        for (int T = 0; T < 2; ++T) { const f32x2 st = stt[16 * T + n]; const float r = st[1] * sc;
            const int pos = sec == 0 ? 16 * T + n : ((n >> 3) << 4) | (T << 3) | (n & 7);
#pragma unroll
            for (int pl = 0; pl < 4; ++pl) { const int pp = (pl + wv) & 3;
                const f32x4 c0 = *(LAS f32x4*)(cwt + (2 * pp) * 16 + 4 * g), c1 = *(LAS f32x4*)(cwt + (2 * pp + 1) * 16 + 4 * g); float v[8];
#pragma unroll
                for (int i = 0; i < 4; ++i) { v[i] = r * (acc[2 * pl][T][i] - st[0] * c0[i]); v[4 + i] = r * (acc[2 * pl + 1][T][i] - st[0] * c1[i]); }
                *(u32x4*)(img + (size_t)((hb + 2 * pp + (g >> 1)) * 64 + tile32) * tb + (g & 1) * 512 + pos * 16) = u32x4{pk2(v[0], v[1]), pk2(v[2], v[3]), pk2(v[4], v[5]), pk2(v[6], v[7])};
            }
            const f32x4 c8 = *(LAS f32x4*)(cwt + 128 + 4 * g); float e[4];
#pragma unroll
            for (int i = 0; i < 4; ++i) e[i] = r * (acc[8][T][i] - st[0] * c8[i]);
            char* xb = img + (size_t)((hb + 2 * g) * 64 + tile32) * tb + 1024 + pos * 4;
            *(unsigned*)xb = pk2(e[0], e[1]); *(unsigned*)(xb + (size_t)64 * tb) = pk2(e[2], e[3]);
        }
    }
}

DI int dslot(int ct, int m) { const int G = ct / 3, t = ct - 3 * G; return 48 * G + 12 * (m >> 2) + 4 * t + (m & 3); }

DI void k1_helper(const unsigned short* wo, char* ws, int hb) {
    const int tid = threadIdx.x, lane = tid & 63, wv = tid >> 6, n = lane & 15, g = lane >> 4;
    for (int fr = hb * 4 + wv; fr < 36 * 18; fr += 256) {
        const int ct = fr / 18, ks = fr - ct * 18, col = dslot(ct, n); unsigned short e[8];
#pragma unroll
        for (int j = 0; j < 8; ++j) { const int row = ks < 16 ? 18 * (2 * ks + (g >> 1)) + 8 * (j >> 2) + 4 * (g & 1) + (j & 3) : 18 * (16 * (ks - 16) + 4 * g + (j >> 1)) + 16 + (j & 1);
            e[j] = wo[row * D + col]; }
        *(u32x4*)(ws + WS_WO + (size_t)fr * 1024 + lane * 16) = u32x4{e[0] | (unsigned)e[1] << 16, e[2] | (unsigned)e[3] << 16, e[4] | (unsigned)e[5] << 16, e[6] | (unsigned)e[7] << 16};
    }
    if (tid < 64) *(u32x4*)(ws + WS_ST + (size_t)hb * 1024 + tid * 16) = u32x4{SENT, SENT, SENT, SENT};
}

__global__ __launch_bounds__(256, 1) void k1_qkv(const char* x, const char* wqkv, const unsigned short* wo, char* ws) {
    extern __shared__ __attribute__((aligned(16))) char smem[];
    const int b = blockIdx.x, xc = b & 7, j = b >> 3;
    if (j >= 24) { k1_helper(wo, ws, (j - 24) * 8 + xc); return; }
    const int rt = (xc >> 1) * 4 + j / 6, cg = (xc & 1) * 6 + j % 6;
    if (cg >= 8) k1_body<true>(x, wqkv, ws, (lptr)smem, rt, cg); else k1_body<false>(x, wqkv, ws, (lptr)smem, rt, cg);
}

constexpr int K2_LDS = 64 * KVT + 256;

__global__ __launch_bounds__(512, 2) void k2_attn(char* ws) {
    extern __shared__ __attribute__((aligned(16))) char smem[];
    const lptr lds = (lptr)smem; const unsigned l0 = (unsigned)(size_t)lds;
    const int tid = threadIdx.x, lane = tid & 63, wv = rfl(tid >> 6), r = lane & 31, hh = lane >> 5;
    const int b = blockIdx.x, head = (b & 7) * 4 + (b >> 6), qt = ((b >> 3) & 7) * 8 + wv;
    const char* qi = ws + WS_QI + (size_t)(head * 64 + qt) * QT;
    const bf16x8 q1 = *(const bf16x8*)(qi + hh * 512 + r * 16);
    const unsigned qx = *(const unsigned*)(qi + 1024 + r * 4);
    const char* kv = ws + WS_KV + (size_t)head * 64 * KVT;
#pragma unroll
    for (int i = 0; i < 18; ++i) glds(kv + i * 8192, wv * 1024 + lane * 16, l0 + i * 8192 + wv * 1024);
    if (wv < 4) glds(kv + 18 * 8192, wv * 1024 + lane * 16, l0 + 18 * 8192 + wv * 1024);
    { constexpr int PER = (int)((E1_BYTES + E2_BYTES) / 256); char* e = ws + WS_E1 + (size_t)b * PER;
      for (int o = tid * 16; o < PER; o += 8192) st128_wt(e, o, u32x4{SENT, SENT, SENT, SENT}); }
    const bf16x8 q2 = __builtin_bit_cast(bf16x8, u32x4{hh ? 0u : qx, 0u, 0u, 0u});
    wait_bar<0>();
    const lptr ka = lds + hh * 512 + r * 16, xa = lds + 1024 + r * 4, va = lds + 1152 + hh * 304 + r * 16;
    f32x16 o = {};
#pragma unroll 2
    for (int t = 0; t < 64; ++t) {
        const bf16x8 kf = lds128(ka + t * KVT); const unsigned kx = *(const LAS unsigned*)(xa + t * KVT);
        const bf16x8 v0 = lds128(va + t * KVT), v1 = lds128(va + t * KVT + 608);
        f32x16 s = MF32(kf, q1, f32x16{});
        s = MF32(__builtin_bit_cast(bf16x8, u32x4{kx, 0u, 0u, 0u}), q2, s);
#pragma unroll
        for (int i = 0; i < 16; ++i) s[i] = __builtin_amdgcn_exp2f(s[i]);
        o = MF32(v0, __builtin_bit_cast(bf16x8, u32x4{pk2(s[0], s[1]), pk2(s[2], s[3]), pk2(s[4], s[5]), pk2(s[6], s[7])}), o);
        o = MF32(v1, __builtin_bit_cast(bf16x8, u32x4{pk2(s[8], s[9]), pk2(s[10], s[11]), pk2(s[12], s[13]), pk2(s[14], s[15])}), o);
    }
    float l = __shfl(o[10], r);
    bool ok = l >= 0x1p-64f && l <= 0x1p64f;
#pragma unroll
    for (int i = 0; i < 8; ++i) ok = ok && fabsf(o[i]) <= 3.0e38f;
    if (!hh) ok = ok && fabsf(o[8]) <= 3.0e38f && fabsf(o[9]) <= 3.0e38f;
    if (__builtin_expect(!__all(ok), 0)) {
        o = f32x16{}; float m = -3.0e38f;
        for (int t = 0; t < 64; ++t) {
            const bf16x8 kf = lds128(ka + t * KVT); const unsigned kx = *(const LAS unsigned*)(xa + t * KVT);
            const bf16x8 v0 = lds128(va + t * KVT), v1 = lds128(va + t * KVT + 608);
            f32x16 s = MF32(kf, q1, f32x16{});
            s = MF32(__builtin_bit_cast(bf16x8, u32x4{kx, 0u, 0u, 0u}), q2, s);
            float mt = s[0];
#pragma unroll
            for (int i = 1; i < 16; ++i) mt = fmaxf(mt, s[i]);
            mt = fmaxf(mt, __shfl_xor(mt, 32)); const float mn = fmaxf(m, mt), al = __builtin_amdgcn_exp2f(m - mn); m = mn;
#pragma unroll
            for (int i = 0; i < 16; ++i) { s[i] = __builtin_amdgcn_exp2f(s[i] - mn); o[i] *= al; }
            o = MF32(v0, __builtin_bit_cast(bf16x8, u32x4{pk2(s[0], s[1]), pk2(s[2], s[3]), pk2(s[4], s[5]), pk2(s[6], s[7])}), o);
            o = MF32(v1, __builtin_bit_cast(bf16x8, u32x4{pk2(s[8], s[9]), pk2(s[10], s[11]), pk2(s[12], s[13]), pk2(s[14], s[15])}), o);
        }
        l = __shfl(o[10], r);
    }
    const float il = 1.f / l;
    char* oi = ws + WS_OI + (size_t)(qt * 2 + (r >> 4)) * 18 * 1024 + (r & 15) * 16;
    *(u32x4*)(oi + (head >> 1) * 1024 + ((head & 1) * 2 + hh) * 256) = u32x4{pk2(o[0] * il, o[1] * il), pk2(o[2] * il, o[3] * il), pk2(o[4] * il, o[5] * il), pk2(o[6] * il, o[7] * il)};
    if (!hh) *(unsigned*)(oi + (16 + (head >> 4)) * 1024 + ((head & 15) >> 2) * 256 + (head & 3) * 4) = pk2(o[8] * il, o[9] * il);
}

constexpr int K3_X = 36864, K3_PART = K3_X + 9216, K3_STAT = K3_PART + 9 * 32 * 8, K3_LDS = K3_STAT + 256;

template <int CTRL> DI float dpp_add(float v) { return v + __builtin_bit_cast(float, __builtin_amdgcn_update_dpp(0, __builtin_bit_cast(int, v), CTRL, 0xF, 0xF, true)); }
DI float row16_sum(float v) { v = dpp_add<0x128>(v); v = dpp_add<0x124>(v); v = dpp_add<0x122>(v); return dpp_add<0x121>(v); }

__global__ __launch_bounds__(576, 1) void k3_oproj(const char* x, char* ws) {
    extern __shared__ __attribute__((aligned(16))) char smem[];
    const lptr lds = (lptr)smem; const unsigned l0 = (unsigned)(size_t)lds;
    const int tid = threadIdx.x, lane = tid & 63, wv = rfl(tid >> 6), n = lane & 15, g = lane >> 4, q = n >> 2, p = lane & 3;
    const int b = blockIdx.x, rg = (b & 7) * 8 + (b >> 5), cq = (b >> 3) & 3, ct = cq * 9 + wv;
    const char* oi = ws + WS_OI + (size_t)rg * 36 * 1024;
#pragma unroll
    for (int i = 0; i < 4; ++i) glds(oi + i * 9216, wv * 1024 + lane * 16, l0 + i * 9216 + wv * 1024);
    { const int row = tid / 18, ch = tid - row * 18; glds(x, (unsigned)((rg * 32 + row) * 1152 + cq * 288 + ch * 16), l0 + K3_X + wv * 1024); }
    const char* wt = ws + WS_WO + (size_t)ct * 18 * 1024 + lane * 16;
    bf16x8 wf[18];
#pragma unroll
    for (int ks = 0; ks < 18; ++ks) wf[ks] = *(const bf16x8*)(wt + ks * 1024);
    wait_bar<18>();
    f32x4 acc[2] = {};
#pragma unroll
    for (int ks = 0; ks < 18; ++ks)
#pragma unroll
        for (int T = 0; T < 2; ++T) acc[T] = MF16(lds128(lds + (T * 18 + ks) * 1024 + lane * 16), wf[ks], acc[T]);
    const int G = wv / 3, t3 = wv - 3 * G;
    float hv[8];
#pragma unroll
    for (int T = 0; T < 2; ++T) { const u32x2 xv = __builtin_bit_cast(u32x2, ldstr(lds + K3_X + (16 * T + 4 * g + q) * 288 + 96 * G + 24 * p + 8 * t3));
        const unsigned a = pk2(acc[T][0] + bflo(xv[0]), acc[T][1] + bfhi(xv[0])), c = pk2(acc[T][2] + bflo(xv[1]), acc[T][3] + bfhi(xv[1]));
        hv[4 * T] = bflo(a); hv[4 * T + 1] = bfhi(a); hv[4 * T + 2] = bflo(c); hv[4 * T + 3] = bfhi(c); }
    LAS f32x2* part = (LAS f32x2*)(lds + K3_PART);
#pragma unroll
    for (int i = 0; i < 8; ++i) { const float s1 = row16_sum(hv[i]), s2 = row16_sum(hv[i] * hv[i]); if (n == 0) part[wv * 32 + 16 * (i >> 2) + 4 * g + (i & 3)] = f32x2{s1, s2}; }
    __syncthreads();
    const int dl = 48 * G + 12 * q + 4 * t3 + p;
#pragma unroll
    for (int i = 0; i < 8; ++i) *(LAS unsigned short*)(lds + K3_X + (16 * (i >> 2) + 4 * g + (i & 3)) * 288 + dl * 2) = (unsigned short)(__float_as_uint(hv[i]) >> 16);
    LAS f32x2* stat = (LAS f32x2*)(lds + K3_STAT);
    if (wv == 0) {
        const int tk = lane & 31; f32x2 a = part[tk];
#pragma unroll
        for (int w = 1; w < 9; ++w) a += part[w * 32 + tk];
        char* st = ws + WS_ST + (size_t)rg * 1024;
        if (lane < 32) st64_wt(st, cq * 256 + tk * 8, u32x2{nosent(__float_as_uint(a[0])), nosent(__float_as_uint(a[1]))});
        u32x2 r1, r2, r3; const unsigned o1 = ((cq + 1) & 3) * 256 + tk * 8, o2 = ((cq + 2) & 3) * 256 + tk * 8, o3 = ((cq + 3) & 3) * 256 + tk * 8;
        for (int sp = 0; sp < SPIN_CAP; ++sp) {
            asm volatile("global_load_dwordx2 %0, %3, %6 sc1\n\tglobal_load_dwordx2 %1, %4, %6 sc1\n\tglobal_load_dwordx2 %2, %5, %6 sc1\n\ts_waitcnt vmcnt(0)"
                         : "=&v"(r1), "=&v"(r2), "=&v"(r3) : "v"(o1), "v"(o2), "v"(o3), "s"(st) : "memory");
            if (__all(r1[0] != SENT && r1[1] != SENT && r2[0] != SENT && r2[1] != SENT && r3[0] != SENT && r3[1] != SENT)) break;
            __builtin_amdgcn_s_sleep(1);
        }
        f32x2 v[4]; const f32x2 f1 = __builtin_bit_cast(f32x2, r1), f2 = __builtin_bit_cast(f32x2, r2), f3 = __builtin_bit_cast(f32x2, r3);
#pragma unroll
        for (int k = 0; k < 4; ++k) { const int dlt = (k - cq) & 3; v[k] = dlt == 0 ? a : dlt == 1 ? f1 : dlt == 2 ? f2 : f3; }
        const f32x2 tt = (v[0] + v[1]) + (v[2] + v[3]);
        const float mean = tt[0] * (1.f / D), var = fmaxf(tt[1] * (1.f / D) - mean * mean, 0.f), ra = rsqrtf(var + LN_EPS);
        const float rb = rsqrtf(var * ra * ra + LN_EPS);
        if (lane < 32) stat[tk] = f32x2{mean, ra * rb};
    }
    __syncthreads();
    { const int row = tid / 18, ch = tid - row * 18; *(u32x4*)(ws + WS_HR + (size_t)(rg * 32 + row) * 1152 + cq * 288 + ch * 16) = *(const LAS u32x4*)(lds + K3_X + tid * 16); }
    float z[8];
#pragma unroll
    for (int i = 0; i < 8; ++i) { const f32x2 s = stat[16 * (i >> 2) + 4 * g + (i & 3)]; z[i] = (hv[i] - s[0]) * s[1]; }
    *(u32x4*)(ws + WS_HN + (size_t)(ct * 64 + rg) * 1024 + lane * 16) = u32x4{pk2(z[0], z[1]), pk2(z[2], z[3]), pk2(z[4], z[5]), pk2(z[6], z[7])};
}

constexpr int GM_RING = 8, GM_LDS = GM_RING * 8192;

DI float gelu_tanh(float u) { const float z = u * (u * u * 0.044715f + 1.f) * (-2.f * 0.7978845608028654f * 1.4426950408889634f); return u * __builtin_amdgcn_rcpf(1.f + __builtin_amdgcn_exp2f(z)); }

template <int PH>
DI void gemm_main(f32x4 (&acc)[3][8], const char* aimg, const char* w, lptr lds, int mt, int nt, int k0, int nk, int rot) {
    constexpr int KST = PH == 0 ? 64 : 144, WROW = PH == 0 ? 9216 : 4096;
    const int tid = threadIdx.x, lane = tid & 63, wv = rfl(tid >> 6), g = lane >> 4, q = (lane & 15) >> 2, p = lane & 3;
    const unsigned l0 = (unsigned)(size_t)lds;
    unsigned wo[2];
#pragma unroll
    for (int u = 0; u < 2; ++u) { const int row = 8 * wv + 4 * u + g, sl = lane & 15; wo[u] = (unsigned)(row * WROW + nt * 256 + ((((sl >> 1) ^ (row & 7)) << 5) | ((sl & 1) << 4))); }
    const char* wb = w + (size_t)k0 * 32 * WROW;
    const char* ab = aimg + ((size_t)(12 * mt + 3 * wv) * KST + k0) * 1024 + lane * 16;
    int ba[8];
#pragma unroll
    for (int i = 0; i < 8; ++i) { const int row = 4 * g + q; ba[i] = row * 256 + (((((i + rot) & 7) ^ (row & 7))) << 5) + 8 * p; }
    auto stage = [&](int t) { const unsigned sl = l0 + (t & (GM_RING - 1)) * 8192 + wv * 2048; const char* src = wb + (size_t)t * 32 * WROW; glds(src, wo[0], sl); glds(src, wo[1], sl + 1024); };
    for (int t = 0; t < GM_RING - 1; ++t) stage(t);
    for (int t = 0; t < nk; ++t) {
        bf16x8 af[3];
#pragma unroll
        for (int i = 0; i < 3; ++i) af[i] = *(const bf16x8*)(ab + ((size_t)i * KST + t) * 1024);
        wait_bar<0>();
        if (t + GM_RING - 1 < nk) stage(t + GM_RING - 1);
        const lptr sl = lds + (t & (GM_RING - 1)) * 8192;
#pragma unroll
        for (int c = 0; c < 8; ++c) { const bf16x8 bf = cat(ldstr(sl + ba[c]), ldstr(sl + ba[c] + 4096));
#pragma unroll
            for (int i = 0; i < 3; ++i) acc[i][c] = PH == 0 ? MF16(bf, af[i], acc[i][c]) : MF16(af[i], bf, acc[i][c]); }
    }
}

__global__ __launch_bounds__(256, 1) void k4_gemm1(const char* w1, const char* b1, char* ws) {
    extern __shared__ __attribute__((aligned(16))) char smem[];
    const int b = blockIdx.x, xc = b & 7, u = b >> 3, j = xc >> 2, nt = (xc & 3) * 9 + u / 3, mt = u % 3;
    const int tid = threadIdx.x, lane = tid & 63, wv = rfl(tid >> 6), g = lane >> 4;
    f32x4 acc[3][8] = {};
    gemm_main<0>(acc, ws + WS_HN, w1, (lptr)smem, mt, nt, 32 * j, 32, 4 * j + 4);
    char* ex = ws + WS_E1 + (size_t)(nt * 3 + mt) * 49152 + wv * 6144;
#pragma unroll
    for (int i = 0; i < 3; ++i)
#pragma unroll
        for (int m = 0; m < 2; ++m) { const f32x4 a = acc[i][2 * m], c = acc[i][2 * m + 1];
            st128_wt(ex + (1 - j) * 24576, (i * 2 + m) * 1024 + lane * 16, u32x4{nosent(pk2(a[0], a[1])), nosent(pk2(a[2], a[3])), nosent(pk2(c[0], c[1])), nosent(pk2(c[2], c[3]))}); }
    u32x2 bs[4];
#pragma unroll
    for (int k = 0; k < 4; ++k) bs[k] = *(const u32x2*)(b1 + (nt * 128 + 64 * j + 16 * k + 4 * g) * 2);
    u32x4 r[6]; const char* rx = ex + j * 24576; const unsigned vo = lane * 16;
    for (int sp = 0; sp < SPIN_CAP; ++sp) {
        asm volatile("global_load_dwordx4 %0, %6, %7 sc1\n\tglobal_load_dwordx4 %1, %6, %7 offset:1024 sc1\n\tglobal_load_dwordx4 %2, %6, %7 offset:2048 sc1\n\t"
                     "global_load_dwordx4 %3, %6, %7 offset:3072 sc1\n\tglobal_load_dwordx4 %4, %6, %8 sc1\n\tglobal_load_dwordx4 %5, %6, %8 offset:1024 sc1\n\ts_waitcnt vmcnt(0)"
                     : "=&v"(r[0]), "=&v"(r[1]), "=&v"(r[2]), "=&v"(r[3]), "=&v"(r[4]), "=&v"(r[5]) : "v"(vo), "s"(rx), "s"(rx + 4096) : "memory");
        bool ok = true;
#pragma unroll
        for (int k = 0; k < 6; ++k) ok = ok && r[k][0] != SENT && r[k][1] != SENT && r[k][2] != SENT && r[k][3] != SENT;
        if (__all(ok)) break;
        __builtin_amdgcn_s_sleep(1);
    }
#pragma unroll
    for (int i = 0; i < 3; ++i)
#pragma unroll
        for (int m = 0; m < 2; ++m) { const f32x4 a = acc[i][4 + 2 * m], c = acc[i][5 + 2 * m]; const u32x4 e = r[i * 2 + m]; const u32x2 ba = bs[2 * m], bc = bs[2 * m + 1];
            const float v0 = gelu_tanh(a[0] + bflo(e[0]) + bflo(ba[0])), v1 = gelu_tanh(a[1] + bfhi(e[0]) + bfhi(ba[0])), v2 = gelu_tanh(a[2] + bflo(e[1]) + bflo(ba[1])), v3 = gelu_tanh(a[3] + bfhi(e[1]) + bfhi(ba[1]));
            const float v4 = gelu_tanh(c[0] + bflo(e[2]) + bflo(bc[0])), v5 = gelu_tanh(c[1] + bfhi(e[2]) + bfhi(bc[0])), v6 = gelu_tanh(c[2] + bflo(e[3]) + bflo(bc[1])), v7 = gelu_tanh(c[3] + bfhi(e[3]) + bfhi(bc[1]));
            *(u32x4*)(ws + WS_UI + ((size_t)(12 * mt + 3 * wv + i) * 144 + nt * 4 + 2 * j + m) * 1024 + lane * 16) = u32x4{pk2(v0, v1), pk2(v2, v3), pk2(v4, v5), pk2(v6, v7)}; }
}

__global__ __launch_bounds__(256, 1) void k5_gemm2(const char* w2, const unsigned short* b2, char* ws, char* out) {
    extern __shared__ __attribute__((aligned(16))) char smem[];
    const int b = blockIdx.x, wl = (b & 7) * 30 + (b >> 3), j = wl / 48, tl = wl - j * 48, nt = tl / 3, mt = tl - nt * 3;
    const int tid = threadIdx.x, lane = tid & 63, wv = rfl(tid >> 6), n = lane & 15, g = lane >> 4;
    f32x4 acc[3][8] = {};
    gemm_main<1>(acc, ws + WS_UI, w2, (lptr)smem, mt, nt, j == 0 ? 0 : 29 * j - 1, j == 0 ? 28 : 29, 0);
    char* ex = ws + WS_E2 + (size_t)tl * (8 * 4 * 4 * 1536) + wv * 1536;
#pragma unroll
    for (int c = 0; c < 8; ++c) { const int own = c < 6 ? c >> 1 : c - 3;
        if (own != j) { const int sl = (j - own + 4) % 5; char* dst = ex + (c * 4 + sl) * 6144;
            st128_wt(dst, lane * 16, u32x4{nosent(pk2(acc[0][c][0], acc[0][c][1])), nosent(pk2(acc[0][c][2], acc[0][c][3])), nosent(pk2(acc[1][c][0], acc[1][c][1])), nosent(pk2(acc[1][c][2], acc[1][c][3]))});
            st64_wt(dst, 1024 + lane * 8, u32x2{nosent(pk2(acc[2][c][0], acc[2][c][1])), nosent(pk2(acc[2][c][2], acc[2][c][3]))}); } }
#pragma unroll
    for (int c = 0; c < 8; ++c) { const int own = c < 6 ? c >> 1 : c - 3;
        if (own == j) {
            const int s = nt * 128 + 16 * c + n; const size_t ro = (size_t)s * 1152 + (192 * mt + 48 * wv + 12 * g) * 2;
            const u32x2 h0 = *(const u32x2*)(ws + WS_HR + ro), h1 = *(const u32x2*)(ws + WS_HR + ro + 8), h2 = *(const u32x2*)(ws + WS_HR + ro + 16);
            const float bias = __uint_as_float((unsigned)b2[s] << 16);
            u32x4 ra[4]; u32x2 rb[4]; const char* rx = ex + c * 4 * 6144; const unsigned v16 = lane * 16, v8 = lane * 8;
            for (int sp = 0; sp < SPIN_CAP; ++sp) {
                asm volatile("global_load_dwordx4 %0, %8, %10 sc1\n\tglobal_load_dwordx2 %4, %9, %10 offset:1024 sc1\n\tglobal_load_dwordx4 %1, %8, %11 sc1\n\tglobal_load_dwordx2 %5, %9, %11 offset:1024 sc1\n\t"
                             "global_load_dwordx4 %2, %8, %12 sc1\n\tglobal_load_dwordx2 %6, %9, %12 offset:1024 sc1\n\tglobal_load_dwordx4 %3, %8, %13 sc1\n\tglobal_load_dwordx2 %7, %9, %13 offset:1024 sc1\n\ts_waitcnt vmcnt(0)"
                             : "=&v"(ra[0]), "=&v"(ra[1]), "=&v"(ra[2]), "=&v"(ra[3]), "=&v"(rb[0]), "=&v"(rb[1]), "=&v"(rb[2]), "=&v"(rb[3])
                             : "v"(v16), "v"(v8), "s"(rx), "s"(rx + 6144), "s"(rx + 12288), "s"(rx + 18432) : "memory");
                bool ok = true;
#pragma unroll
                for (int k = 0; k < 4; ++k) ok = ok && ra[k][0] != SENT && ra[k][1] != SENT && ra[k][2] != SENT && ra[k][3] != SENT && rb[k][0] != SENT && rb[k][1] != SENT;
                if (__all(ok)) break;
                __builtin_amdgcn_s_sleep(1);
            }
            float v[12];
#pragma unroll
            for (int i = 0; i < 4; ++i) { v[i] = acc[0][c][i]; v[4 + i] = acc[1][c][i]; v[8 + i] = acc[2][c][i]; }
#pragma unroll
            for (int k = 0; k < 4; ++k) {
#pragma unroll
                for (int e = 0; e < 4; ++e) { v[2 * e] += bflo(ra[k][e]); v[2 * e + 1] += bfhi(ra[k][e]); }
#pragma unroll
                for (int e = 0; e < 2; ++e) { v[8 + 2 * e] += bflo(rb[k][e]); v[9 + 2 * e] += bfhi(rb[k][e]); }
            }
            const unsigned hw[6] = {h0[0], h0[1], h1[0], h1[1], h2[0], h2[1]}; unsigned ow[6];
#pragma unroll
            for (int e = 0; e < 6; ++e) ow[e] = pk2(v[2 * e] + bias + bflo(hw[e]), v[2 * e + 1] + bias + bfhi(hw[e]));
            *(u32x2*)(out + ro) = u32x2{ow[0], ow[1]}; *(u32x2*)(out + ro + 8) = u32x2{ow[2], ow[3]}; *(u32x2*)(out + ro + 16) = u32x2{ow[4], ow[5]};
        } }
}

extern "C" void kernel_launch(void* const* d_in, const int* in_sizes, int n_in, void* d_out, int out_size, void* d_ws, size_t ws_size, hipStream_t stream) {
    const char* x = (const char*)d_in[0]; const char* wqkv = (const char*)d_in[1]; const unsigned short* wo = (const unsigned short*)d_in[2];
    const char* w1 = (const char*)d_in[4]; const char* b1 = (const char*)d_in[5]; const char* w2 = (const char*)d_in[6]; const unsigned short* b2 = (const unsigned short*)d_in[7];
    char* ws = (char*)d_ws;
    (void)hipFuncSetAttribute((const void*)k1_qkv, hipFuncAttributeMaxDynamicSharedMemorySize, K1_LDS);
    (void)hipFuncSetAttribute((const void*)k2_attn, hipFuncAttributeMaxDynamicSharedMemorySize, K2_LDS);
    (void)hipFuncSetAttribute((const void*)k4_gemm1, hipFuncAttributeMaxDynamicSharedMemorySize, GM_LDS);
    (void)hipFuncSetAttribute((const void*)k5_gemm2, hipFuncAttributeMaxDynamicSharedMemorySize, GM_LDS);
    k1_qkv<<<256, 256, K1_LDS, stream>>>(x, wqkv, wo, ws);
    k2_attn<<<256, 512, K2_LDS, stream>>>(ws);
    k3_oproj<<<256, 576, K3_LDS, stream>>>(x, ws);
    k4_gemm1<<<216, 256, GM_LDS, stream>>>(w1, b1, ws);
    k5_gemm2<<<240, 256, GM_LDS, stream>>>(w2, b2, ws, (char*)d_out);
}
```

```cpp
#include <hip/hip_runtime.h>
#include <hip/hip_bf16.h>
#include <stdint.h>

typedef short bf16x8 __attribute__((ext_vector_type(8)));
typedef short s16x4 __attribute__((ext_vector_type(4)));
typedef float f32x2 __attribute__((ext_vector_type(2)));
typedef float f32x4 __attribute__((ext_vector_type(4)));
typedef float f32x8 __attribute__((ext_vector_type(8)));
typedef float f32x16 __attribute__((ext_vector_type(16)));
typedef float f32x32 __attribute__((ext_vector_type(32)));
typedef unsigned u32x2 __attribute__((ext_vector_type(2)));
typedef unsigned u32x3 __attribute__((ext_vector_type(3)));
typedef unsigned u32x4 __attribute__((ext_vector_type(4)));
typedef unsigned u32x8 __attribute__((ext_vector_type(8)));
typedef unsigned u32x32 __attribute__((ext_vector_type(32)));
typedef __bf16 bf2_t __attribute__((ext_vector_type(2)));
typedef unsigned long long u64;
#define LAS __attribute__((address_space(3)))
#define DI __device__ __forceinline__
typedef LAS char* lptr;

constexpr int S = 2048, D = 576, H = 32, F = 4608;
constexpr float LN_EPS = 1e-5f;
constexpr float QSCALE = 0.23570226039551584f * 1.4426950408889634f;
constexpr unsigned SENT = 0xFFFFFFFFu, OOB = 0x40000000u, LOOB = 0x40000u;
constexpr int SPIN_CAP = 1 << 15;

constexpr int PAD = 2304;
constexpr int QT = 1152, KVT = 2432;
constexpr int QHS = 64 * QT + PAD, KHS = 64 * KVT + PAD, ORS = 36864 + PAD, E1S = 49152, E2S = 196608;
constexpr size_t WS_QI  = 0;
constexpr size_t WS_KV  = WS_QI + (size_t)H * QHS;
constexpr size_t WS_OI  = WS_KV + (size_t)H * KHS;
constexpr size_t WS_WO  = WS_OI + (size_t)64 * ORS;
constexpr size_t WS_HN  = WS_WO + (size_t)36 * 18 * 1024;
constexpr int HNS = 65536 + PAD, UIS = 147456;
constexpr size_t WS_HR  = WS_HN + (size_t)36 * HNS;
constexpr size_t WS_UI  = WS_HR + (size_t)S * D * 2;
constexpr size_t WS_ST  = WS_UI + (size_t)36 * UIS;
constexpr size_t WS_E1  = WS_ST + 65536;
constexpr size_t E1_BYTES = (size_t)108 * E1S;
constexpr size_t WS_E2  = WS_E1 + E1_BYTES;
constexpr size_t E2_BYTES = (size_t)48 * E2S;
constexpr size_t WS_T1  = WS_E2 + E2_BYTES;
constexpr int T1_BYTES  = 16384 + 65536;
constexpr size_t WS_FL  = WS_T1 + T1_BYTES;
constexpr size_t WS_EP  = WS_FL + 5 * 256 * 128;
constexpr size_t WS_END = WS_EP + 128;

constexpr int K1_SLOT = 16384 + 18432, K1_TAB = 4 * K1_SLOT;
constexpr int L_RING = 0, L_RING2 = 65536;
constexpr int L_O = 65536, L_PART = 131072, L_STAT = L_PART + 1280;
constexpr int L_X = L_PART + 2048, L_JUNK = 64 * KVT, LDS_BYTES = L_JUNK + 256;

constexpr int D1 = 14, D2 = 16, D3 = 18, D4 = 12, DP = 1, DS = 6, DE1 = 6, DE2A = 10;
#define TMASK(w) ((w) == 0 ? 0x0F8 : (w) == 1 ? 0x178 : (w) == 2 ? 0x1A8 : 0x1C8)


#define ATT_ASM \
"v_mov_b32 v0, 0\n" \
"v_mov_b32 v1, 0\n" \
"v_mov_b32 v2, 0\n" \
"v_mov_b32 v3, 0\n" \
"v_mov_b32 v4, 0\n" \
"v_mov_b32 v5, 0\n" \
"v_mov_b32 v6, 0\n" \
"v_mov_b32 v7, 0\n" \
"v_mov_b32 v8, 0\n" \
"v_mov_b32 v9, 0\n" \
"v_mov_b32 v10, 0\n" \
"v_mov_b32 v11, 0\n" \
"v_mov_b32 v12, 0\n" \
"v_mov_b32 v13, 0\n" \
"v_mov_b32 v14, 0\n" \
"v_mov_b32 v15, 0\n" \
"v_mov_b32 v16, 0\n" \
"v_mov_b32 v17, 0\n" \
"v_mov_b32 v18, 0\n" \
"v_mov_b32 v19, 0\n" \
"v_mov_b32 v20, 0\n" \
"v_mov_b32 v21, 0\n" \
"v_mov_b32 v22, 0\n" \
"v_mov_b32 v23, 0\n" \
"v_mov_b32 v24, 0\n" \
"v_mov_b32 v25, 0\n" \
"v_mov_b32 v26, 0\n" \
"v_mov_b32 v27, 0\n" \
"v_mov_b32 v28, 0\n" \
"v_mov_b32 v29, 0\n" \
"v_mov_b32 v30, 0\n" \
"v_mov_b32 v31, 0\n" \
"v_mov_b32 v32, 0\n" \
"v_mov_b32 v33, 0\n" \
"v_mov_b32 v34, 0\n" \
"v_mov_b32 v35, 0\n" \
"v_mov_b32 v36, 0\n" \
"v_mov_b32 v37, 0\n" \
"v_mov_b32 v38, 0\n" \
"v_mov_b32 v39, 0\n" \
"v_mov_b32 v40, 0\n" \
"v_mov_b32 v41, 0\n" \
"v_mov_b32 v42, 0\n" \
"v_mov_b32 v43, 0\n" \
"v_mov_b32 v44, 0\n" \
"v_mov_b32 v45, 0\n" \
"v_mov_b32 v46, 0\n" \
"v_mov_b32 v47, 0\n" \
"v_mov_b32 v48, 0\n" \
"v_mov_b32 v49, 0\n" \
"v_mov_b32 v50, 0\n" \
"v_mov_b32 v51, 0\n" \
"v_mov_b32 v52, 0\n" \
"v_mov_b32 v53, 0\n" \
"v_mov_b32 v54, 0\n" \
"v_mov_b32 v55, 0\n" \
"v_mov_b32 v56, 0\n" \
"v_mov_b32 v57, 0\n" \
"v_mov_b32 v58, 0\n" \
"v_mov_b32 v59, 0\n" \
"v_mov_b32 v60, 0\n" \
"v_mov_b32 v61, 0\n" \
"v_mov_b32 v62, 0\n" \
"v_mov_b32 v63, 0\n" \
"v_mov_b32 v96, 0\n" \
"v_mov_b32 v97, 0\n" \
"v_mov_b32 v98, 0\n" \
"v_mov_b32 v99, 0\n" \
"v_mov_b32 v100, 0\n" \
"v_mov_b32 v101, 0\n" \
"v_mov_b32 v102, 0\n" \
"v_mov_b32 v103, 0\n" \
"v_mov_b32 v80, 0\n" \
"v_mov_b32 v81, 0\n" \
"v_mov_b32 v82, 0\n" \
"v_mov_b32 v83, 0\n" \
"v_mov_b32 v84, 0\n" \
"v_mov_b32 v85, 0\n" \
"v_mov_b32 v86, 0\n" \
"v_mov_b32 v87, 0\n" \
"v_mov_b32 v88, 0\n" \
"v_mov_b32 v89, 0\n" \
"v_mov_b32 v90, 0\n" \
"v_mov_b32 v91, 0\n" \
"v_mov_b32 v92, 0\n" \
"v_mov_b32 v93, 0\n" \
"v_mov_b32 v94, 0\n" \
"v_mov_b32 v95, 0\n" \
"v_mov_b32 v109, 0\n" \
"v_mov_b32 v110, 0\n" \
"v_mov_b32 v111, 0\n" \
"v_mov_b32 v125, 0\n" \
"v_mov_b32 v126, 0\n" \
"v_mov_b32 v127, 0\n" \
"v_mov_b32 v153, 0\n" \
"v_mov_b32 v154, 0\n" \
"v_mov_b32 v155, 0\n" \
"v_mov_b32 v157, 0\n" \
"v_mov_b32 v158, 0\n" \
"v_mov_b32 v159, 0\n" \
"v_mov_b32 v161, 0\n" \
"v_mov_b32 v162, 0\n" \
"v_mov_b32 v163, 0\n" \
"v_mov_b32 v165, 0\n" \
"v_mov_b32 v166, 0\n" \
"v_mov_b32 v167, 0\n" \
"v_mov_b32 v128, 0\n" \
"v_mov_b32 v129, 0\n" \
"v_mov_b32 v130, 0\n" \
"v_mov_b32 v131, 0\n" \
"v_mov_b32 v132, 0\n" \
"v_mov_b32 v133, 0\n" \
"v_mov_b32 v134, 0\n" \
"v_mov_b32 v135, 0\n" \
"s_mov_b32 s60, 8\n" \
"s_barrier\n" \
"s_add_u32 m0, s56, 0\n" \
"s_add_u32 s59, s58, 0\n" \
"buffer_load_dwordx4 v173, s[40:43], s59 offen sc1 lds\n" \
"s_add_u32 m0, s56, 77824\n" \
"s_add_u32 s59, s58, 77824\n" \
"buffer_load_dwordx4 v173, s[40:43], s59 offen sc1 lds\n" \
"s_add_u32 m0, s56, 4096\n" \
"s_add_u32 s59, s58, 4096\n" \
"buffer_load_dwordx4 v173, s[40:43], s59 offen sc1 lds\n" \
"s_add_u32 m0, s56, 81920\n" \
"s_add_u32 s59, s58, 81920\n" \
"buffer_load_dwordx4 v173, s[40:43], s59 offen sc1 lds\n" \
"s_add_u32 m0, s56, 8192\n" \
"s_add_u32 s59, s58, 8192\n" \
"buffer_load_dwordx4 v173, s[40:43], s59 offen sc1 lds\n" \
"s_add_u32 m0, s56, 86016\n" \
"s_add_u32 s59, s58, 86016\n" \
"buffer_load_dwordx4 v173, s[40:43], s59 offen sc1 lds\n" \
"buffer_load_dwordx4 v[136:139], v171, s[44:47], s48 offen sc1\n" \
"buffer_load_dword v152, v172, s[44:47], s48 offen offset:1024 sc1\n" \
"buffer_load_dwordx4 v[140:143], v171, s[44:47], s49 offen sc1\n" \
"buffer_load_dword v156, v172, s[44:47], s49 offen offset:1024 sc1\n" \
"buffer_load_dwordx4 v[144:147], v171, s[44:47], s50 offen sc1\n" \
"buffer_load_dword v160, v172, s[44:47], s50 offen offset:1024 sc1\n" \
"buffer_load_dwordx4 v[148:151], v171, s[44:47], s51 offen sc1\n" \
"buffer_load_dword v164, v172, s[44:47], s51 offen offset:1024 sc1\n" \
"s_waitcnt vmcnt(8)\n" \
"s_barrier\n" \
"ds_read_b128 v[104:107], v168 offset:0\n" \
"ds_read_b32 v108, v169 offset:0\n" \
"ds_read_b128 v[112:115], v170 offset:0\n" \
"ds_read_b128 v[116:119], v170 offset:640\n" \
"s_waitcnt vmcnt(0) lgkmcnt(0)\n" \
"v_mfma_f32_32x32x16_bf16 v[64:79], v[104:107], v[136:139], 0\n" \
"v_mfma_f32_32x32x8bf16_1k v[64:79], v[108:109], v[152:153], v[64:79]\n" \
"s_nop 7\n" \
"s_nop 7\n" \
"s_branch Latt_%=_go\n" \
"Latt_%=_loop:\n" \
"s_cmp_lt_u32 s60, 5\n" \
"s_cbranch_scc1 Latt_%=_go\n" \
"s_barrier\n" \
"Latt_%=_go:\n" \
"s_bitcmp1_b32 s52, s60\n" \
"s_cbranch_scc0 Latt_%=_nt\n" \
"s_bitcmp1_b32 s53, 0\n" \
"s_cselect_b64 s[64:65], s[68:69], s[70:71]\n" \
"s_mov_b32 m0, s55\n" \
"s_lshr_b32 s53, s53, 1\n" \
"buffer_load_dword v174, s[64:67], s54 offen lds\n" \
"s_mov_b32 s54, s57\n" \
"s_mov_b32 s57, s61\n" \
"s_mov_b32 s61, s62\n" \
"s_mov_b32 s62, s63\n" \
"Latt_%=_nt:\n" \
"v_mfma_f32_32x32x16_bf16 v[48:63], v[128:131], v[96:99], v[48:63]\n" \
"v_cvt_pk_bf16_f32 v102, v92, v93\n" \
"v_cvt_pk_bf16_f32 v103, v94, v95\n" \
"v_exp_f32 v64, v64\n" \
"v_exp_f32 v65, v65\n" \
"v_exp_f32 v66, v66\n" \
"v_exp_f32 v67, v67\n" \
"v_mfma_f32_32x32x16_bf16 v[80:95], v[104:107], v[140:143], 0\n" \
"v_cvt_pk_bf16_f32 v96, v64, v65\n" \
"v_cvt_pk_bf16_f32 v97, v66, v67\n" \
"v_exp_f32 v68, v68\n" \
"v_exp_f32 v69, v69\n" \
"v_exp_f32 v70, v70\n" \
"v_exp_f32 v71, v71\n" \
"v_mfma_f32_32x32x8bf16_1k v[80:95], v[108:109], v[156:157], v[80:95]\n" \
"v_cvt_pk_bf16_f32 v98, v68, v69\n" \
"v_cvt_pk_bf16_f32 v99, v70, v71\n" \
"v_exp_f32 v72, v72\n" \
"v_exp_f32 v73, v73\n" \
"v_exp_f32 v74, v74\n" \
"v_exp_f32 v75, v75\n" \
"v_mfma_f32_32x32x16_bf16 v[48:63], v[132:135], v[100:103], v[48:63]\n" \
"v_cvt_pk_bf16_f32 v100, v72, v73\n" \
"v_cvt_pk_bf16_f32 v101, v74, v75\n" \
"v_exp_f32 v76, v76\n" \
"v_exp_f32 v77, v77\n" \
"v_exp_f32 v78, v78\n" \
"v_exp_f32 v79, v79\n" \
"v_mfma_f32_32x32x16_bf16 v[0:15], v[112:115], v[96:99], v[0:15]\n" \
"ds_read_b128 v[120:123], v168 offset:2432\n" \
"v_cvt_pk_bf16_f32 v102, v76, v77\n" \
"v_cvt_pk_bf16_f32 v103, v78, v79\n" \
"v_exp_f32 v80, v80\n" \
"v_exp_f32 v81, v81\n" \
"v_exp_f32 v82, v82\n" \
"v_exp_f32 v83, v83\n" \
"v_mfma_f32_32x32x16_bf16 v[64:79], v[104:107], v[144:147], 0\n" \
"ds_read_b32 v124, v169 offset:2432\n" \
"v_cvt_pk_bf16_f32 v96, v80, v81\n" \
"v_cvt_pk_bf16_f32 v97, v82, v83\n" \
"v_exp_f32 v84, v84\n" \
"v_exp_f32 v85, v85\n" \
"v_exp_f32 v86, v86\n" \
"v_exp_f32 v87, v87\n" \
"v_mfma_f32_32x32x8bf16_1k v[64:79], v[108:109], v[160:161], v[64:79]\n" \
"ds_read_b128 v[128:131], v170 offset:2432\n" \
"v_cvt_pk_bf16_f32 v98, v84, v85\n" \
"v_cvt_pk_bf16_f32 v99, v86, v87\n" \
"v_exp_f32 v88, v88\n" \
"v_exp_f32 v89, v89\n" \
"v_exp_f32 v90, v90\n" \
"v_exp_f32 v91, v91\n" \
"v_mfma_f32_32x32x16_bf16 v[0:15], v[116:119], v[100:103], v[0:15]\n" \
"ds_read_b128 v[132:135], v170 offset:3072\n" \
"v_cvt_pk_bf16_f32 v100, v88, v89\n" \
"v_cvt_pk_bf16_f32 v101, v90, v91\n" \
"v_exp_f32 v92, v92\n" \
"v_exp_f32 v93, v93\n" \
"v_exp_f32 v94, v94\n" \
"v_exp_f32 v95, v95\n" \
"v_mfma_f32_32x32x16_bf16 v[16:31], v[112:115], v[96:99], v[16:31]\n" \
"v_cvt_pk_bf16_f32 v102, v92, v93\n" \
"v_cvt_pk_bf16_f32 v103, v94, v95\n" \
"v_exp_f32 v64, v64\n" \
"v_exp_f32 v65, v65\n" \
"v_exp_f32 v66, v66\n" \
"v_exp_f32 v67, v67\n" \
"v_mfma_f32_32x32x16_bf16 v[80:95], v[104:107], v[148:151], 0\n" \
"v_cvt_pk_bf16_f32 v96, v64, v65\n" \
"v_cvt_pk_bf16_f32 v97, v66, v67\n" \
"v_exp_f32 v68, v68\n" \
"v_exp_f32 v69, v69\n" \
"v_exp_f32 v70, v70\n" \
"v_exp_f32 v71, v71\n" \
"v_mfma_f32_32x32x8bf16_1k v[80:95], v[108:109], v[164:165], v[80:95]\n" \
"v_cvt_pk_bf16_f32 v98, v68, v69\n" \
"v_cvt_pk_bf16_f32 v99, v70, v71\n" \
"v_exp_f32 v72, v72\n" \
"v_exp_f32 v73, v73\n" \
"v_exp_f32 v74, v74\n" \
"v_exp_f32 v75, v75\n" \
"v_mfma_f32_32x32x16_bf16 v[16:31], v[116:119], v[100:103], v[16:31]\n" \
"v_cvt_pk_bf16_f32 v100, v72, v73\n" \
"v_cvt_pk_bf16_f32 v101, v74, v75\n" \
"v_exp_f32 v76, v76\n" \
"v_exp_f32 v77, v77\n" \
"v_exp_f32 v78, v78\n" \
"v_exp_f32 v79, v79\n" \
"v_mfma_f32_32x32x16_bf16 v[32:47], v[112:115], v[96:99], v[32:47]\n" \
"v_cvt_pk_bf16_f32 v102, v76, v77\n" \
"v_cvt_pk_bf16_f32 v103, v78, v79\n" \
"v_exp_f32 v80, v80\n" \
"v_exp_f32 v81, v81\n" \
"v_exp_f32 v82, v82\n" \
"v_exp_f32 v83, v83\n" \
"s_waitcnt lgkmcnt(0)\n" \
"v_mfma_f32_32x32x16_bf16 v[64:79], v[120:123], v[136:139], 0\n" \
"v_cvt_pk_bf16_f32 v96, v80, v81\n" \
"v_cvt_pk_bf16_f32 v97, v82, v83\n" \
"v_exp_f32 v84, v84\n" \
"v_exp_f32 v85, v85\n" \
"v_exp_f32 v86, v86\n" \
"v_exp_f32 v87, v87\n" \
"v_mfma_f32_32x32x8bf16_1k v[64:79], v[124:125], v[152:153], v[64:79]\n" \
"v_cvt_pk_bf16_f32 v98, v84, v85\n" \
"v_cvt_pk_bf16_f32 v99, v86, v87\n" \
"v_exp_f32 v88, v88\n" \
"v_exp_f32 v89, v89\n" \
"v_exp_f32 v90, v90\n" \
"v_exp_f32 v91, v91\n" \
"v_mfma_f32_32x32x16_bf16 v[32:47], v[116:119], v[100:103], v[32:47]\n" \
"v_cvt_pk_bf16_f32 v100, v88, v89\n" \
"v_cvt_pk_bf16_f32 v101, v90, v91\n" \
"v_exp_f32 v92, v92\n" \
"v_exp_f32 v93, v93\n" \
"v_exp_f32 v94, v94\n" \
"v_exp_f32 v95, v95\n" \
"v_mfma_f32_32x32x16_bf16 v[48:63], v[112:115], v[96:99], v[48:63]\n" \
"v_cvt_pk_bf16_f32 v102, v92, v93\n" \
"v_cvt_pk_bf16_f32 v103, v94, v95\n" \
"v_exp_f32 v64, v64\n" \
"v_exp_f32 v65, v65\n" \
"v_exp_f32 v66, v66\n" \
"v_exp_f32 v67, v67\n" \
"v_mfma_f32_32x32x16_bf16 v[80:95], v[120:123], v[140:143], 0\n" \
"v_cvt_pk_bf16_f32 v96, v64, v65\n" \
"v_cvt_pk_bf16_f32 v97, v66, v67\n" \
"v_exp_f32 v68, v68\n" \
"v_exp_f32 v69, v69\n" \
"v_exp_f32 v70, v70\n" \
"v_exp_f32 v71, v71\n" \
"v_mfma_f32_32x32x8bf16_1k v[80:95], v[124:125], v[156:157], v[80:95]\n" \
"v_cvt_pk_bf16_f32 v98, v68, v69\n" \
"v_cvt_pk_bf16_f32 v99, v70, v71\n" \
"v_exp_f32 v72, v72\n" \
"v_exp_f32 v73, v73\n" \
"v_exp_f32 v74, v74\n" \
"v_exp_f32 v75, v75\n" \
"v_mfma_f32_32x32x16_bf16 v[48:63], v[116:119], v[100:103], v[48:63]\n" \
"v_cvt_pk_bf16_f32 v100, v72, v73\n" \
"v_cvt_pk_bf16_f32 v101, v74, v75\n" \
"v_exp_f32 v76, v76\n" \
"v_exp_f32 v77, v77\n" \
"v_exp_f32 v78, v78\n" \
"v_exp_f32 v79, v79\n" \
"v_mfma_f32_32x32x16_bf16 v[0:15], v[128:131], v[96:99], v[0:15]\n" \
"ds_read_b128 v[104:107], v168 offset:4864\n" \
"v_cvt_pk_bf16_f32 v102, v76, v77\n" \
"v_cvt_pk_bf16_f32 v103, v78, v79\n" \
"v_exp_f32 v80, v80\n" \
"v_exp_f32 v81, v81\n" \
"v_exp_f32 v82, v82\n" \
"v_exp_f32 v83, v83\n" \
"v_mfma_f32_32x32x16_bf16 v[64:79], v[120:123], v[144:147], 0\n" \
"ds_read_b32 v108, v169 offset:4864\n" \
"v_cvt_pk_bf16_f32 v96, v80, v81\n" \
"v_cvt_pk_bf16_f32 v97, v82, v83\n" \
"v_exp_f32 v84, v84\n" \
"v_exp_f32 v85, v85\n" \
"v_exp_f32 v86, v86\n" \
"v_exp_f32 v87, v87\n" \
"v_mfma_f32_32x32x8bf16_1k v[64:79], v[124:125], v[160:161], v[64:79]\n" \
"ds_read_b128 v[112:115], v170 offset:4864\n" \
"v_cvt_pk_bf16_f32 v98, v84, v85\n" \
"v_cvt_pk_bf16_f32 v99, v86, v87\n" \
"v_exp_f32 v88, v88\n" \
"v_exp_f32 v89, v89\n" \
"v_exp_f32 v90, v90\n" \
"v_exp_f32 v91, v91\n" \
"v_mfma_f32_32x32x16_bf16 v[0:15], v[132:135], v[100:103], v[0:15]\n" \
"ds_read_b128 v[116:119], v170 offset:5504\n" \
"v_cvt_pk_bf16_f32 v100, v88, v89\n" \
"v_cvt_pk_bf16_f32 v101, v90, v91\n" \
"v_exp_f32 v92, v92\n" \
"v_exp_f32 v93, v93\n" \
"v_exp_f32 v94, v94\n" \
"v_exp_f32 v95, v95\n" \
"v_mfma_f32_32x32x16_bf16 v[16:31], v[128:131], v[96:99], v[16:31]\n" \
"v_cvt_pk_bf16_f32 v102, v92, v93\n" \
"v_cvt_pk_bf16_f32 v103, v94, v95\n" \
"v_exp_f32 v64, v64\n" \
"v_exp_f32 v65, v65\n" \
"v_exp_f32 v66, v66\n" \
"v_exp_f32 v67, v67\n" \
"v_mfma_f32_32x32x16_bf16 v[80:95], v[120:123], v[148:151], 0\n" \
"v_cvt_pk_bf16_f32 v96, v64, v65\n" \
"v_cvt_pk_bf16_f32 v97, v66, v67\n" \
"v_exp_f32 v68, v68\n" \
"v_exp_f32 v69, v69\n" \
"v_exp_f32 v70, v70\n" \
"v_exp_f32 v71, v71\n" \
"v_mfma_f32_32x32x8bf16_1k v[80:95], v[124:125], v[164:165], v[80:95]\n" \
"v_cvt_pk_bf16_f32 v98, v68, v69\n" \
"v_cvt_pk_bf16_f32 v99, v70, v71\n" \
"v_exp_f32 v72, v72\n" \
"v_exp_f32 v73, v73\n" \
"v_exp_f32 v74, v74\n" \
"v_exp_f32 v75, v75\n" \
"v_mfma_f32_32x32x16_bf16 v[16:31], v[132:135], v[100:103], v[16:31]\n" \
"v_cvt_pk_bf16_f32 v100, v72, v73\n" \
"v_cvt_pk_bf16_f32 v101, v74, v75\n" \
"v_exp_f32 v76, v76\n" \
"v_exp_f32 v77, v77\n" \
"v_exp_f32 v78, v78\n" \
"v_exp_f32 v79, v79\n" \
"v_mfma_f32_32x32x16_bf16 v[32:47], v[128:131], v[96:99], v[32:47]\n" \
"v_cvt_pk_bf16_f32 v102, v76, v77\n" \
"v_cvt_pk_bf16_f32 v103, v78, v79\n" \
"v_exp_f32 v80, v80\n" \
"v_exp_f32 v81, v81\n" \
"v_exp_f32 v82, v82\n" \
"v_exp_f32 v83, v83\n" \
"s_waitcnt lgkmcnt(0)\n" \
"v_mfma_f32_32x32x16_bf16 v[64:79], v[104:107], v[136:139], 0\n" \
"v_cvt_pk_bf16_f32 v96, v80, v81\n" \
"v_cvt_pk_bf16_f32 v97, v82, v83\n" \
"v_exp_f32 v84, v84\n" \
"v_exp_f32 v85, v85\n" \
"v_exp_f32 v86, v86\n" \
"v_exp_f32 v87, v87\n" \
"v_mfma_f32_32x32x8bf16_1k v[64:79], v[108:109], v[152:153], v[64:79]\n" \
"v_cvt_pk_bf16_f32 v98, v84, v85\n" \
"v_cvt_pk_bf16_f32 v99, v86, v87\n" \
"v_exp_f32 v88, v88\n" \
"v_exp_f32 v89, v89\n" \
"v_exp_f32 v90, v90\n" \
"v_exp_f32 v91, v91\n" \
"v_mfma_f32_32x32x16_bf16 v[32:47], v[132:135], v[100:103], v[32:47]\n" \
"v_cvt_pk_bf16_f32 v100, v88, v89\n" \
"v_cvt_pk_bf16_f32 v101, v90, v91\n" \
"v_exp_f32 v92, v92\n" \
"v_exp_f32 v93, v93\n" \
"v_exp_f32 v94, v94\n" \
"v_exp_f32 v95, v95\n" \
"v_mfma_f32_32x32x16_bf16 v[48:63], v[128:131], v[96:99], v[48:63]\n" \
"v_cvt_pk_bf16_f32 v102, v92, v93\n" \
"v_cvt_pk_bf16_f32 v103, v94, v95\n" \
"v_exp_f32 v64, v64\n" \
"v_exp_f32 v65, v65\n" \
"v_exp_f32 v66, v66\n" \
"v_exp_f32 v67, v67\n" \
"v_mfma_f32_32x32x16_bf16 v[80:95], v[104:107], v[140:143], 0\n" \
"v_cvt_pk_bf16_f32 v96, v64, v65\n" \
"v_cvt_pk_bf16_f32 v97, v66, v67\n" \
"v_exp_f32 v68, v68\n" \
"v_exp_f32 v69, v69\n" \
"v_exp_f32 v70, v70\n" \
"v_exp_f32 v71, v71\n" \
"v_mfma_f32_32x32x8bf16_1k v[80:95], v[108:109], v[156:157], v[80:95]\n" \
"v_cvt_pk_bf16_f32 v98, v68, v69\n" \
"v_cvt_pk_bf16_f32 v99, v70, v71\n" \
"v_exp_f32 v72, v72\n" \
"v_exp_f32 v73, v73\n" \
"v_exp_f32 v74, v74\n" \
"v_exp_f32 v75, v75\n" \
"v_mfma_f32_32x32x16_bf16 v[48:63], v[132:135], v[100:103], v[48:63]\n" \
"v_cvt_pk_bf16_f32 v100, v72, v73\n" \
"v_cvt_pk_bf16_f32 v101, v74, v75\n" \
"v_exp_f32 v76, v76\n" \
"v_exp_f32 v77, v77\n" \
"v_exp_f32 v78, v78\n" \
"v_exp_f32 v79, v79\n" \
"v_mfma_f32_32x32x16_bf16 v[0:15], v[112:115], v[96:99], v[0:15]\n" \
"ds_read_b128 v[120:123], v168 offset:7296\n" \
"v_cvt_pk_bf16_f32 v102, v76, v77\n" \
"v_cvt_pk_bf16_f32 v103, v78, v79\n" \
"v_exp_f32 v80, v80\n" \
"v_exp_f32 v81, v81\n" \
"v_exp_f32 v82, v82\n" \
"v_exp_f32 v83, v83\n" \
"v_mfma_f32_32x32x16_bf16 v[64:79], v[104:107], v[144:147], 0\n" \
"ds_read_b32 v124, v169 offset:7296\n" \
"v_cvt_pk_bf16_f32 v96, v80, v81\n" \
"v_cvt_pk_bf16_f32 v97, v82, v83\n" \
"v_exp_f32 v84, v84\n" \
"v_exp_f32 v85, v85\n" \
"v_exp_f32 v86, v86\n" \
"v_exp_f32 v87, v87\n" \
"v_mfma_f32_32x32x8bf16_1k v[64:79], v[108:109], v[160:161], v[64:79]\n" \
"ds_read_b128 v[128:131], v170 offset:7296\n" \
"v_cvt_pk_bf16_f32 v98, v84, v85\n" \
"v_cvt_pk_bf16_f32 v99, v86, v87\n" \
"v_exp_f32 v88, v88\n" \
"v_exp_f32 v89, v89\n" \
"v_exp_f32 v90, v90\n" \
"v_exp_f32 v91, v91\n" \
"v_mfma_f32_32x32x16_bf16 v[0:15], v[116:119], v[100:103], v[0:15]\n" \
"ds_read_b128 v[132:135], v170 offset:7936\n" \
"v_cvt_pk_bf16_f32 v100, v88, v89\n" \
"v_cvt_pk_bf16_f32 v101, v90, v91\n" \
"v_exp_f32 v92, v92\n" \
"v_exp_f32 v93, v93\n" \
"v_exp_f32 v94, v94\n" \
"v_exp_f32 v95, v95\n" \
"v_mfma_f32_32x32x16_bf16 v[16:31], v[112:115], v[96:99], v[16:31]\n" \
"v_cvt_pk_bf16_f32 v102, v92, v93\n" \
"v_cvt_pk_bf16_f32 v103, v94, v95\n" \
"v_exp_f32 v64, v64\n" \
"v_exp_f32 v65, v65\n" \
"v_exp_f32 v66, v66\n" \
"v_exp_f32 v67, v67\n" \
"v_mfma_f32_32x32x16_bf16 v[80:95], v[104:107], v[148:151], 0\n" \
"v_cvt_pk_bf16_f32 v96, v64, v65\n" \
"v_cvt_pk_bf16_f32 v97, v66, v67\n" \
"v_exp_f32 v68, v68\n" \
"v_exp_f32 v69, v69\n" \
"v_exp_f32 v70, v70\n" \
"v_exp_f32 v71, v71\n" \
"v_mfma_f32_32x32x8bf16_1k v[80:95], v[108:109], v[164:165], v[80:95]\n" \
"v_cvt_pk_bf16_f32 v98, v68, v69\n" \
"v_cvt_pk_bf16_f32 v99, v70, v71\n" \
"v_exp_f32 v72, v72\n" \
"v_exp_f32 v73, v73\n" \
"v_exp_f32 v74, v74\n" \
"v_exp_f32 v75, v75\n" \
"v_mfma_f32_32x32x16_bf16 v[16:31], v[116:119], v[100:103], v[16:31]\n" \
"v_cvt_pk_bf16_f32 v100, v72, v73\n" \
"v_cvt_pk_bf16_f32 v101, v74, v75\n" \
"v_exp_f32 v76, v76\n" \
"v_exp_f32 v77, v77\n" \
"v_exp_f32 v78, v78\n" \
"v_exp_f32 v79, v79\n" \
"v_mfma_f32_32x32x16_bf16 v[32:47], v[112:115], v[96:99], v[32:47]\n" \
"v_cvt_pk_bf16_f32 v102, v76, v77\n" \
"v_cvt_pk_bf16_f32 v103, v78, v79\n" \
"v_exp_f32 v80, v80\n" \
"v_exp_f32 v81, v81\n" \
"v_exp_f32 v82, v82\n" \
"v_exp_f32 v83, v83\n" \
"s_waitcnt lgkmcnt(0)\n" \
"v_mfma_f32_32x32x16_bf16 v[64:79], v[120:123], v[136:139], 0\n" \
"v_cvt_pk_bf16_f32 v96, v80, v81\n" \
"v_cvt_pk_bf16_f32 v97, v82, v83\n" \
"v_exp_f32 v84, v84\n" \
"v_exp_f32 v85, v85\n" \
"v_exp_f32 v86, v86\n" \
"v_exp_f32 v87, v87\n" \
"v_mfma_f32_32x32x8bf16_1k v[64:79], v[124:125], v[152:153], v[64:79]\n" \
"v_cvt_pk_bf16_f32 v98, v84, v85\n" \
"v_cvt_pk_bf16_f32 v99, v86, v87\n" \
"v_exp_f32 v88, v88\n" \
"v_exp_f32 v89, v89\n" \
"v_exp_f32 v90, v90\n" \
"v_exp_f32 v91, v91\n" \
"v_mfma_f32_32x32x16_bf16 v[32:47], v[116:119], v[100:103], v[32:47]\n" \
"v_cvt_pk_bf16_f32 v100, v88, v89\n" \
"v_cvt_pk_bf16_f32 v101, v90, v91\n" \
"v_exp_f32 v92, v92\n" \
"v_exp_f32 v93, v93\n" \
"v_exp_f32 v94, v94\n" \
"v_exp_f32 v95, v95\n" \
"v_mfma_f32_32x32x16_bf16 v[48:63], v[112:115], v[96:99], v[48:63]\n" \
"v_cvt_pk_bf16_f32 v102, v92, v93\n" \
"v_cvt_pk_bf16_f32 v103, v94, v95\n" \
"v_exp_f32 v64, v64\n" \
"v_exp_f32 v65, v65\n" \
"v_exp_f32 v66, v66\n" \
"v_exp_f32 v67, v67\n" \
"v_mfma_f32_32x32x16_bf16 v[80:95], v[120:123], v[140:143], 0\n" \
"v_cvt_pk_bf16_f32 v96, v64, v65\n" \
"v_cvt_pk_bf16_f32 v97, v66, v67\n" \
"v_exp_f32 v68, v68\n" \
"v_exp_f32 v69, v69\n" \
"v_exp_f32 v70, v70\n" \
"v_exp_f32 v71, v71\n" \
"v_mfma_f32_32x32x8bf16_1k v[80:95], v[124:125], v[156:157], v[80:95]\n" \
"v_cvt_pk_bf16_f32 v98, v68, v69\n" \
"v_cvt_pk_bf16_f32 v99, v70, v71\n" \
"v_exp_f32 v72, v72\n" \
"v_exp_f32 v73, v73\n" \
"v_exp_f32 v74, v74\n" \
"v_exp_f32 v75, v75\n" \
"v_mfma_f32_32x32x16_bf16 v[48:63], v[116:119], v[100:103], v[48:63]\n" \
"v_cvt_pk_bf16_f32 v100, v72, v73\n" \
"v_cvt_pk_bf16_f32 v101, v74, v75\n" \
"v_exp_f32 v76, v76\n" \
"v_exp_f32 v77, v77\n" \
"v_exp_f32 v78, v78\n" \
"v_exp_f32 v79, v79\n" \
"v_mfma_f32_32x32x16_bf16 v[0:15], v[128:131], v[96:99], v[0:15]\n" \
"ds_read_b128 v[104:107], v168 offset:9728\n" \
"v_cvt_pk_bf16_f32 v102, v76, v77\n" \
"v_cvt_pk_bf16_f32 v103, v78, v79\n" \
"v_exp_f32 v80, v80\n" \
"v_exp_f32 v81, v81\n" \
"v_exp_f32 v82, v82\n" \
"v_exp_f32 v83, v83\n" \
"v_mfma_f32_32x32x16_bf16 v[64:79], v[120:123], v[144:147], 0\n" \
"ds_read_b32 v108, v169 offset:9728\n" \
"v_cvt_pk_bf16_f32 v96, v80, v81\n" \
"v_cvt_pk_bf16_f32 v97, v82, v83\n" \
"v_exp_f32 v84, v84\n" \
"v_exp_f32 v85, v85\n" \
"v_exp_f32 v86, v86\n" \
"v_exp_f32 v87, v87\n" \
"v_mfma_f32_32x32x8bf16_1k v[64:79], v[124:125], v[160:161], v[64:79]\n" \
"ds_read_b128 v[112:115], v170 offset:9728\n" \
"v_cvt_pk_bf16_f32 v98, v84, v85\n" \
"v_cvt_pk_bf16_f32 v99, v86, v87\n" \
"v_exp_f32 v88, v88\n" \
"v_exp_f32 v89, v89\n" \
"v_exp_f32 v90, v90\n" \
"v_exp_f32 v91, v91\n" \
"v_mfma_f32_32x32x16_bf16 v[0:15], v[132:135], v[100:103], v[0:15]\n" \
"ds_read_b128 v[116:119], v170 offset:10368\n" \
"v_cvt_pk_bf16_f32 v100, v88, v89\n" \
"v_cvt_pk_bf16_f32 v101, v90, v91\n" \
"v_exp_f32 v92, v92\n" \
"v_exp_f32 v93, v93\n" \
"v_exp_f32 v94, v94\n" \
"v_exp_f32 v95, v95\n" \
"v_mfma_f32_32x32x16_bf16 v[16:31], v[128:131], v[96:99], v[16:31]\n" \
"v_add_u32 v168, 9728, v168\n" \
"v_cvt_pk_bf16_f32 v102, v92, v93\n" \
"v_cvt_pk_bf16_f32 v103, v94, v95\n" \
"v_exp_f32 v64, v64\n" \
"v_exp_f32 v65, v65\n" \
"v_exp_f32 v66, v66\n" \
"v_exp_f32 v67, v67\n" \
"v_mfma_f32_32x32x16_bf16 v[80:95], v[120:123], v[148:151], 0\n" \
"v_add_u32 v169, 9728, v169\n" \
"v_cvt_pk_bf16_f32 v96, v64, v65\n" \
"v_cvt_pk_bf16_f32 v97, v66, v67\n" \
"v_exp_f32 v68, v68\n" \
"v_exp_f32 v69, v69\n" \
"v_exp_f32 v70, v70\n" \
"v_exp_f32 v71, v71\n" \
"v_mfma_f32_32x32x8bf16_1k v[80:95], v[124:125], v[164:165], v[80:95]\n" \
"v_add_u32 v170, 9728, v170\n" \
"v_cvt_pk_bf16_f32 v98, v68, v69\n" \
"v_cvt_pk_bf16_f32 v99, v70, v71\n" \
"v_exp_f32 v72, v72\n" \
"v_exp_f32 v73, v73\n" \
"v_exp_f32 v74, v74\n" \
"v_exp_f32 v75, v75\n" \
"v_mfma_f32_32x32x16_bf16 v[16:31], v[132:135], v[100:103], v[16:31]\n" \
"v_cvt_pk_bf16_f32 v100, v72, v73\n" \
"v_cvt_pk_bf16_f32 v101, v74, v75\n" \
"v_exp_f32 v76, v76\n" \
"v_exp_f32 v77, v77\n" \
"v_exp_f32 v78, v78\n" \
"v_exp_f32 v79, v79\n" \
"v_mfma_f32_32x32x16_bf16 v[32:47], v[128:131], v[96:99], v[32:47]\n" \
"s_sub_u32 s60, s60, 1\n" \
"v_cvt_pk_bf16_f32 v102, v76, v77\n" \
"v_cvt_pk_bf16_f32 v103, v78, v79\n" \
"v_exp_f32 v80, v80\n" \
"v_exp_f32 v81, v81\n" \
"v_exp_f32 v82, v82\n" \
"v_exp_f32 v83, v83\n" \
"s_waitcnt lgkmcnt(0)\n" \
"v_mfma_f32_32x32x16_bf16 v[64:79], v[104:107], v[136:139], 0\n" \
"s_cmp_eq_u32 s60, 0\n" \
"v_cvt_pk_bf16_f32 v96, v80, v81\n" \
"v_cvt_pk_bf16_f32 v97, v82, v83\n" \
"v_exp_f32 v84, v84\n" \
"v_exp_f32 v85, v85\n" \
"v_exp_f32 v86, v86\n" \
"v_exp_f32 v87, v87\n" \
"s_cbranch_scc0 Latt_%=_ns\n" \
"ds_write_b128 v175, v[0:3] offset:0\n" \
"ds_write_b128 v175, v[4:7] offset:1024\n" \
"ds_write_b128 v175, v[8:11] offset:2048\n" \
"ds_write_b128 v175, v[16:19] offset:3072\n" \
"ds_write_b128 v175, v[20:23] offset:4096\n" \
"ds_write_b128 v175, v[24:27] offset:5120\n" \
"Latt_%=_ns:\n" \
"v_mfma_f32_32x32x8bf16_1k v[64:79], v[108:109], v[152:153], v[64:79]\n" \
"v_cvt_pk_bf16_f32 v98, v84, v85\n" \
"v_cvt_pk_bf16_f32 v99, v86, v87\n" \
"v_exp_f32 v88, v88\n" \
"v_exp_f32 v89, v89\n" \
"v_exp_f32 v90, v90\n" \
"v_exp_f32 v91, v91\n" \
"v_mfma_f32_32x32x16_bf16 v[32:47], v[132:135], v[100:103], v[32:47]\n" \
"v_cvt_pk_bf16_f32 v100, v88, v89\n" \
"v_cvt_pk_bf16_f32 v101, v90, v91\n" \
"v_exp_f32 v92, v92\n" \
"v_exp_f32 v93, v93\n" \
"v_exp_f32 v94, v94\n" \
"v_exp_f32 v95, v95\n" \
"s_cbranch_scc0 Latt_%=_loop\n" \
"v_mfma_f32_32x32x16_bf16 v[48:63], v[128:131], v[96:99], v[48:63]\n" \
"v_cvt_pk_bf16_f32 v102, v92, v93\n" \
"v_cvt_pk_bf16_f32 v103, v94, v95\n" \
"s_waitcnt lgkmcnt(0)\n" \
"s_nop 0\n" \
"v_mfma_f32_32x32x16_bf16 v[48:63], v[132:135], v[100:103], v[48:63]\n" \
"s_barrier\n" \
"ds_read_b128 v[64:67], v176 offset:0\n" \
"ds_read_b128 v[68:71], v176 offset:1024\n" \
"ds_read_b128 v[72:75], v176 offset:2048\n" \
"ds_read_b128 v[76:79], v176 offset:3072\n" \
"ds_read_b128 v[80:83], v176 offset:4096\n" \
"ds_read_b128 v[84:87], v176 offset:5120\n" \
"s_mov_b32 s59, 0x1f800000\n" \
"s_mov_b32 s53, 0x5f800000\n" \
"s_mov_b32 s52, 0x7f61b1e6\n" \
"s_mov_b64 s[64:65], exec\n" \
"s_waitcnt lgkmcnt(3)\n" \
"v_add_f32 v96, v42, v74\n" \
"v_pk_add_f32 v[32:33], v[32:33], v[64:65]\n" \
"v_pk_add_f32 v[34:35], v[34:35], v[66:67]\n" \
"v_pk_add_f32 v[36:37], v[36:37], v[68:69]\n" \
"v_pk_add_f32 v[38:39], v[38:39], v[70:71]\n" \
"v_pk_add_f32 v[40:41], v[40:41], v[72:73]\n" \
"v_mov_b32 v97, v96\n" \
"v_add_f32 v100, abs(v32), abs(v33)\n" \
"v_add_f32 v100, abs(v34), v100\n" \
"v_permlane32_swap_b32 v96, v97\n" \
"v_add_f32 v100, abs(v35), v100\n" \
"v_add_f32 v100, abs(v36), v100\n" \
"v_add_f32 v100, abs(v37), v100\n" \
"v_add_f32 v100, abs(v38), v100\n" \
"v_add_f32 v100, abs(v39), v100\n" \
"v_add_f32 v100, abs(v40), v100\n" \
"v_add_f32 v100, abs(v41), v100\n" \
"v_rcp_f32 v97, v96\n" \
"v_cmp_le_f32 vcc, s59, v96\n" \
"s_and_b64 s[64:65], s[64:65], vcc\n" \
"v_cmp_ge_f32 vcc, s53, v96\n" \
"s_and_b64 s[64:65], s[64:65], vcc\n" \
"v_cmp_ge_f32 vcc, s52, v100\n" \
"s_and_b64 s[64:65], s[64:65], vcc\n" \
"v_mul_f32 v32, v97, v32\n" \
"v_mul_f32 v33, v97, v33\n" \
"v_mul_f32 v34, v97, v34\n" \
"v_mul_f32 v35, v97, v35\n" \
"v_mul_f32 v36, v97, v36\n" \
"v_mul_f32 v37, v97, v37\n" \
"v_mul_f32 v38, v97, v38\n" \
"v_mul_f32 v39, v97, v39\n" \
"v_mul_f32 v40, v97, v40\n" \
"v_mul_f32 v41, v97, v41\n" \
"v_cvt_pk_bf16_f32 v64, v32, v33\n" \
"v_cvt_pk_bf16_f32 v65, v34, v35\n" \
"v_cvt_pk_bf16_f32 v66, v36, v37\n" \
"v_cvt_pk_bf16_f32 v67, v38, v39\n" \
"v_cvt_pk_bf16_f32 v68, v40, v41\n" \
"buffer_store_dwordx4 v[64:67], v177, s[72:75], s76 offen sc1\n" \
"buffer_store_dword v68, v178, s[72:75], s77 offen sc1\n" \
"s_waitcnt lgkmcnt(0)\n" \
"s_nop 1\n" \
"v_add_f32 v98, v58, v86\n" \
"v_pk_add_f32 v[48:49], v[48:49], v[76:77]\n" \
"v_pk_add_f32 v[50:51], v[50:51], v[78:79]\n" \
"v_pk_add_f32 v[52:53], v[52:53], v[80:81]\n" \
"v_pk_add_f32 v[54:55], v[54:55], v[82:83]\n" \
"v_pk_add_f32 v[56:57], v[56:57], v[84:85]\n" \
"v_mov_b32 v99, v98\n" \
"v_add_f32 v101, abs(v48), abs(v49)\n" \
"v_add_f32 v101, abs(v50), v101\n" \
"v_permlane32_swap_b32 v98, v99\n" \
"v_add_f32 v101, abs(v51), v101\n" \
"v_add_f32 v101, abs(v52), v101\n" \
"v_add_f32 v101, abs(v53), v101\n" \
"v_add_f32 v101, abs(v54), v101\n" \
"v_add_f32 v101, abs(v55), v101\n" \
"v_add_f32 v101, abs(v56), v101\n" \
"v_add_f32 v101, abs(v57), v101\n" \
"v_rcp_f32 v99, v98\n" \
"v_cmp_le_f32 vcc, s59, v98\n" \
"s_and_b64 s[64:65], s[64:65], vcc\n" \
"v_cmp_ge_f32 vcc, s53, v98\n" \
"s_and_b64 s[64:65], s[64:65], vcc\n" \
"v_cmp_ge_f32 vcc, s52, v101\n" \
"s_and_b64 s[64:65], s[64:65], vcc\n" \
"v_mul_f32 v48, v99, v48\n" \
"v_mul_f32 v49, v99, v49\n" \
"v_mul_f32 v50, v99, v50\n" \
"v_mul_f32 v51, v99, v51\n" \
"v_mul_f32 v52, v99, v52\n" \
"v_mul_f32 v53, v99, v53\n" \
"v_mul_f32 v54, v99, v54\n" \
"v_mul_f32 v55, v99, v55\n" \
"v_mul_f32 v56, v99, v56\n" \
"v_mul_f32 v57, v99, v57\n" \
"v_cvt_pk_bf16_f32 v76, v48, v49\n" \
"v_cvt_pk_bf16_f32 v77, v50, v51\n" \
"v_cvt_pk_bf16_f32 v78, v52, v53\n" \
"v_cvt_pk_bf16_f32 v79, v54, v55\n" \
"v_cvt_pk_bf16_f32 v80, v56, v57\n" \
"buffer_store_dwordx4 v[76:79], v177, s[72:75], s78 offen sc1\n" \
"buffer_store_dword v80, v178, s[72:75], s79 offen sc1\n" \
"s_cmp_eq_u64 s[64:65], exec\n" \
"s_cselect_b32 s53, 1, 0\n"
#define ATT_CLOB "v0","v1","v2","v3","v4","v5","v6","v7","v8","v9","v10","v11","v12","v13","v14","v15","v16","v17","v18","v19","v20","v21","v22","v23","v24","v25","v26","v27","v28","v29","v30","v31","v32","v33","v34","v35","v36","v37","v38","v39","v40","v41","v42","v43","v44","v45","v46","v47","v48","v49","v50","v51","v52","v53","v54","v55","v56","v57","v58","v59","v60","v61","v62","v63","v64","v65","v66","v67","v68","v69","v70","v71","v72","v73","v74","v75","v76","v77","v78","v79","v80","v81","v82","v83","v84","v85","v86","v87","v88","v89","v90","v91","v92","v93","v94","v95","v96","v97","v98","v99","v100","v101","v102","v103","v104","v105","v106","v107","v108","v109","v110","v111","v112","v113","v114","v115","v116","v117","v118","v119","v120","v121","v122","v123","v124","v125","v126","v127","v128","v129","v130","v131","v132","v133","v134","v135","v136","v137","v138","v139","v140","v141","v142","v143","v144","v145","v146","v147","v148","v149","v150","v151","v152","v153","v154","v155","v156","v157","v158","v159","v160","v161","v162","v163","v164","v165","v166","v167","s59","s60","vcc","scc","memory"

#define GEMM1_ASM \
"s_mov_b32 s59, 0\n" \
"s_mov_b32 s60, 3\n" \
"ds_read_b64_tr_b16 v[96:97], v224 offset:0\n" \
"ds_read_b64_tr_b16 v[98:99], v224 offset:4096\n" \
"ds_read_b64_tr_b16 v[100:101], v225 offset:0\n" \
"ds_read_b64_tr_b16 v[102:103], v225 offset:4096\n" \
"ds_read_b64_tr_b16 v[104:105], v226 offset:0\n" \
"ds_read_b64_tr_b16 v[106:107], v226 offset:4096\n" \
"ds_read_b64_tr_b16 v[108:109], v227 offset:0\n" \
"ds_read_b64_tr_b16 v[110:111], v227 offset:4096\n" \
"ds_read_b64_tr_b16 v[112:113], v228 offset:0\n" \
"ds_read_b64_tr_b16 v[114:115], v228 offset:4096\n" \
"ds_read_b64_tr_b16 v[116:117], v229 offset:0\n" \
"ds_read_b64_tr_b16 v[118:119], v229 offset:4096\n" \
"ds_read_b64_tr_b16 v[120:121], v230 offset:0\n" \
"ds_read_b64_tr_b16 v[122:123], v230 offset:4096\n" \
"ds_read_b64_tr_b16 v[124:125], v231 offset:0\n" \
"ds_read_b64_tr_b16 v[126:127], v231 offset:4096\n" \
"v_mov_b32 v0, 0\n" \
"v_mov_b32 v1, 0\n" \
"v_mov_b32 v2, 0\n" \
"v_mov_b32 v3, 0\n" \
"v_mov_b32 v4, 0\n" \
"v_mov_b32 v5, 0\n" \
"v_mov_b32 v6, 0\n" \
"v_mov_b32 v7, 0\n" \
"v_mov_b32 v8, 0\n" \
"v_mov_b32 v9, 0\n" \
"v_mov_b32 v10, 0\n" \
"v_mov_b32 v11, 0\n" \
"v_mov_b32 v12, 0\n" \
"v_mov_b32 v13, 0\n" \
"v_mov_b32 v14, 0\n" \
"v_mov_b32 v15, 0\n" \
"v_mov_b32 v16, v238\n" \
"v_mov_b32 v17, v239\n" \
"v_mov_b32 v18, v240\n" \
"v_mov_b32 v19, v241\n" \
"v_mov_b32 v20, v242\n" \
"v_mov_b32 v21, v243\n" \
"v_mov_b32 v22, v244\n" \
"v_mov_b32 v23, v245\n" \
"v_mov_b32 v24, v246\n" \
"v_mov_b32 v25, v247\n" \
"v_mov_b32 v26, v248\n" \
"v_mov_b32 v27, v249\n" \
"v_mov_b32 v28, v250\n" \
"v_mov_b32 v29, v251\n" \
"v_mov_b32 v30, v252\n" \
"v_mov_b32 v31, v253\n" \
"v_mov_b32 v32, 0\n" \
"v_mov_b32 v33, 0\n" \
"v_mov_b32 v34, 0\n" \
"v_mov_b32 v35, 0\n" \
"v_mov_b32 v36, 0\n" \
"v_mov_b32 v37, 0\n" \
"v_mov_b32 v38, 0\n" \
"v_mov_b32 v39, 0\n" \
"v_mov_b32 v40, 0\n" \
"v_mov_b32 v41, 0\n" \
"v_mov_b32 v42, 0\n" \
"v_mov_b32 v43, 0\n" \
"v_mov_b32 v44, 0\n" \
"v_mov_b32 v45, 0\n" \
"v_mov_b32 v46, 0\n" \
"v_mov_b32 v47, 0\n" \
"v_mov_b32 v48, v238\n" \
"v_mov_b32 v49, v239\n" \
"v_mov_b32 v50, v240\n" \
"v_mov_b32 v51, v241\n" \
"v_mov_b32 v52, v242\n" \
"v_mov_b32 v53, v243\n" \
"v_mov_b32 v54, v244\n" \
"v_mov_b32 v55, v245\n" \
"v_mov_b32 v56, v246\n" \
"v_mov_b32 v57, v247\n" \
"v_mov_b32 v58, v248\n" \
"v_mov_b32 v59, v249\n" \
"v_mov_b32 v60, v250\n" \
"v_mov_b32 v61, v251\n" \
"v_mov_b32 v62, v252\n" \
"v_mov_b32 v63, v253\n" \
"v_mov_b32 v64, 0\n" \
"v_mov_b32 v65, 0\n" \
"v_mov_b32 v66, 0\n" \
"v_mov_b32 v67, 0\n" \
"v_mov_b32 v68, 0\n" \
"v_mov_b32 v69, 0\n" \
"v_mov_b32 v70, 0\n" \
"v_mov_b32 v71, 0\n" \
"v_mov_b32 v72, 0\n" \
"v_mov_b32 v73, 0\n" \
"v_mov_b32 v74, 0\n" \
"v_mov_b32 v75, 0\n" \
"v_mov_b32 v76, 0\n" \
"v_mov_b32 v77, 0\n" \
"v_mov_b32 v78, 0\n" \
"v_mov_b32 v79, 0\n" \
"v_mov_b32 v80, v238\n" \
"v_mov_b32 v81, v239\n" \
"v_mov_b32 v82, v240\n" \
"v_mov_b32 v83, v241\n" \
"v_mov_b32 v84, v242\n" \
"v_mov_b32 v85, v243\n" \
"v_mov_b32 v86, v244\n" \
"v_mov_b32 v87, v245\n" \
"v_mov_b32 v88, v246\n" \
"v_mov_b32 v89, v247\n" \
"v_mov_b32 v90, v248\n" \
"v_mov_b32 v91, v249\n" \
"v_mov_b32 v92, v250\n" \
"v_mov_b32 v93, v251\n" \
"v_mov_b32 v94, v252\n" \
"v_mov_b32 v95, v253\n" \
"s_barrier\n" \
"buffer_load_dwordx4 v[128:131], v232, s[44:47], s59 offen sc1\n" \
"buffer_load_dwordx4 v[132:135], v232, s[48:51], s59 offen sc1\n" \
"buffer_load_dwordx4 v[136:139], v232, s[52:55], s59 offen sc1\n" \
"buffer_load_dwordx4 v[140:143], v232, s[44:47], s59 offen offset:1024 sc1\n" \
"buffer_load_dwordx4 v[144:147], v232, s[48:51], s59 offen offset:1024 sc1\n" \
"buffer_load_dwordx4 v[148:151], v232, s[52:55], s59 offen offset:1024 sc1\n" \
"buffer_load_dwordx4 v[152:155], v232, s[44:47], s59 offen offset:2048 sc1\n" \
"buffer_load_dwordx4 v[156:159], v232, s[48:51], s59 offen offset:2048 sc1\n" \
"buffer_load_dwordx4 v[160:163], v232, s[52:55], s59 offen offset:2048 sc1\n" \
"s_add_u32 s59, s59, 3072\n" \
"s_waitcnt vmcnt(6)\n" \
"Lg1_%=_loop:\n" \
"s_waitcnt vmcnt(10) lgkmcnt(8)\n" \
"s_barrier\n" \
"v_mfma_f32_16x16x32_bf16 v[0:3], v[96:99], v[128:131], v[0:3]\n" \
"s_add_u32 m0, s56, 49152\n" \
"v_mfma_f32_16x16x32_bf16 v[32:35], v[96:99], v[132:135], v[32:35]\n" \
"buffer_load_dwordx4 v234, s[40:43], s58 offen lds\n" \
"v_mfma_f32_16x16x32_bf16 v[64:67], v[96:99], v[136:139], v[64:67]\n" \
"ds_read_b64_tr_b16 v[96:97], v224 offset:8192\n" \
"ds_read_b64_tr_b16 v[98:99], v224 offset:12288\n" \
"v_mfma_f32_16x16x32_bf16 v[4:7], v[100:103], v[128:131], v[4:7]\n" \
"v_mfma_f32_16x16x32_bf16 v[36:39], v[100:103], v[132:135], v[36:39]\n" \
"v_mfma_f32_16x16x32_bf16 v[68:71], v[100:103], v[136:139], v[68:71]\n" \
"ds_read_b64_tr_b16 v[100:101], v225 offset:8192\n" \
"ds_read_b64_tr_b16 v[102:103], v225 offset:12288\n" \
"v_mfma_f32_16x16x32_bf16 v[8:11], v[104:107], v[128:131], v[8:11]\n" \
"buffer_load_dwordx4 v235, s[40:43], s58 offen offset:1024 lds\n" \
"v_mfma_f32_16x16x32_bf16 v[40:43], v[104:107], v[132:135], v[40:43]\n" \
"s_add_u32 s58, s58, s61\n" \
"v_mfma_f32_16x16x32_bf16 v[72:75], v[104:107], v[136:139], v[72:75]\n" \
"ds_read_b64_tr_b16 v[104:105], v226 offset:8192\n" \
"ds_read_b64_tr_b16 v[106:107], v226 offset:12288\n" \
"v_mfma_f32_16x16x32_bf16 v[12:15], v[108:111], v[128:131], v[12:15]\n" \
"v_mfma_f32_16x16x32_bf16 v[44:47], v[108:111], v[132:135], v[44:47]\n" \
"buffer_load_dwordx4 v[164:167], v232, s[44:47], s59 offen sc1\n" \
"v_mfma_f32_16x16x32_bf16 v[76:79], v[108:111], v[136:139], v[76:79]\n" \
"ds_read_b64_tr_b16 v[108:109], v227 offset:8192\n" \
"ds_read_b64_tr_b16 v[110:111], v227 offset:12288\n" \
"s_waitcnt lgkmcnt(8)\n" \
"v_mfma_f32_16x16x32_bf16 v[16:19], v[112:115], v[128:131], v[16:19]\n" \
"v_mfma_f32_16x16x32_bf16 v[48:51], v[112:115], v[132:135], v[48:51]\n" \
"v_mfma_f32_16x16x32_bf16 v[80:83], v[112:115], v[136:139], v[80:83]\n" \
"ds_read_b64_tr_b16 v[112:113], v228 offset:8192\n" \
"ds_read_b64_tr_b16 v[114:115], v228 offset:12288\n" \
"v_mfma_f32_16x16x32_bf16 v[20:23], v[116:119], v[128:131], v[20:23]\n" \
"buffer_load_dwordx4 v[168:171], v232, s[48:51], s59 offen sc1\n" \
"v_mfma_f32_16x16x32_bf16 v[52:55], v[116:119], v[132:135], v[52:55]\n" \
"v_mfma_f32_16x16x32_bf16 v[84:87], v[116:119], v[136:139], v[84:87]\n" \
"ds_read_b64_tr_b16 v[116:117], v229 offset:8192\n" \
"ds_read_b64_tr_b16 v[118:119], v229 offset:12288\n" \
"v_mfma_f32_16x16x32_bf16 v[24:27], v[120:123], v[128:131], v[24:27]\n" \
"v_mfma_f32_16x16x32_bf16 v[56:59], v[120:123], v[132:135], v[56:59]\n" \
"buffer_load_dwordx4 v[172:175], v232, s[52:55], s59 offen sc1\n" \
"v_mfma_f32_16x16x32_bf16 v[88:91], v[120:123], v[136:139], v[88:91]\n" \
"ds_read_b64_tr_b16 v[120:121], v230 offset:8192\n" \
"ds_read_b64_tr_b16 v[122:123], v230 offset:12288\n" \
"v_mfma_f32_16x16x32_bf16 v[28:31], v[124:127], v[128:131], v[28:31]\n" \
"s_add_u32 s59, s59, 1024\n" \
"v_mfma_f32_16x16x32_bf16 v[60:63], v[124:127], v[132:135], v[60:63]\n" \
"v_mfma_f32_16x16x32_bf16 v[92:95], v[124:127], v[136:139], v[92:95]\n" \
"ds_read_b64_tr_b16 v[124:125], v231 offset:8192\n" \
"ds_read_b64_tr_b16 v[126:127], v231 offset:12288\n" \
"s_waitcnt vmcnt(8) lgkmcnt(8)\n" \
"v_mfma_f32_16x16x32_bf16 v[0:3], v[96:99], v[140:143], v[0:3]\n" \
"s_add_u32 m0, s56, 57344\n" \
"v_mfma_f32_16x16x32_bf16 v[32:35], v[96:99], v[144:147], v[32:35]\n" \
"buffer_load_dwordx4 v234, s[40:43], s58 offen lds\n" \
"v_mfma_f32_16x16x32_bf16 v[64:67], v[96:99], v[148:151], v[64:67]\n" \
"ds_read_b64_tr_b16 v[96:97], v224 offset:16384\n" \
"ds_read_b64_tr_b16 v[98:99], v224 offset:20480\n" \
"v_mfma_f32_16x16x32_bf16 v[4:7], v[100:103], v[140:143], v[4:7]\n" \
"v_mfma_f32_16x16x32_bf16 v[36:39], v[100:103], v[144:147], v[36:39]\n" \
"v_mfma_f32_16x16x32_bf16 v[68:71], v[100:103], v[148:151], v[68:71]\n" \
"ds_read_b64_tr_b16 v[100:101], v225 offset:16384\n" \
"ds_read_b64_tr_b16 v[102:103], v225 offset:20480\n" \
"v_mfma_f32_16x16x32_bf16 v[8:11], v[104:107], v[140:143], v[8:11]\n" \
"buffer_load_dwordx4 v235, s[40:43], s58 offen offset:1024 lds\n" \
"v_mfma_f32_16x16x32_bf16 v[40:43], v[104:107], v[144:147], v[40:43]\n" \
"s_add_u32 s58, s58, s61\n" \
"v_mfma_f32_16x16x32_bf16 v[72:75], v[104:107], v[148:151], v[72:75]\n" \
"ds_read_b64_tr_b16 v[104:105], v226 offset:16384\n" \
"ds_read_b64_tr_b16 v[106:107], v226 offset:20480\n" \
"v_mfma_f32_16x16x32_bf16 v[12:15], v[108:111], v[140:143], v[12:15]\n" \
"v_mfma_f32_16x16x32_bf16 v[44:47], v[108:111], v[144:147], v[44:47]\n" \
"buffer_load_dwordx4 v[176:179], v232, s[44:47], s59 offen sc1\n" \
"v_mfma_f32_16x16x32_bf16 v[76:79], v[108:111], v[148:151], v[76:79]\n" \
"ds_read_b64_tr_b16 v[108:109], v227 offset:16384\n" \
"ds_read_b64_tr_b16 v[110:111], v227 offset:20480\n" \
"s_waitcnt lgkmcnt(8)\n" \
"v_mfma_f32_16x16x32_bf16 v[16:19], v[112:115], v[140:143], v[16:19]\n" \
"v_mfma_f32_16x16x32_bf16 v[48:51], v[112:115], v[144:147], v[48:51]\n" \
"v_mfma_f32_16x16x32_bf16 v[80:83], v[112:115], v[148:151], v[80:83]\n" \
"ds_read_b64_tr_b16 v[112:113], v228 offset:16384\n" \
"ds_read_b64_tr_b16 v[114:115], v228 offset:20480\n" \
"v_mfma_f32_16x16x32_bf16 v[20:23], v[116:119], v[140:143], v[20:23]\n" \
"buffer_load_dwordx4 v[180:183], v232, s[48:51], s59 offen sc1\n" \
"v_mfma_f32_16x16x32_bf16 v[52:55], v[116:119], v[144:147], v[52:55]\n" \
"v_mfma_f32_16x16x32_bf16 v[84:87], v[116:119], v[148:151], v[84:87]\n" \
"ds_read_b64_tr_b16 v[116:117], v229 offset:16384\n" \
"ds_read_b64_tr_b16 v[118:119], v229 offset:20480\n" \
"v_mfma_f32_16x16x32_bf16 v[24:27], v[120:123], v[140:143], v[24:27]\n" \
"v_mfma_f32_16x16x32_bf16 v[56:59], v[120:123], v[144:147], v[56:59]\n" \
"buffer_load_dwordx4 v[184:187], v232, s[52:55], s59 offen sc1\n" \
"v_mfma_f32_16x16x32_bf16 v[88:91], v[120:123], v[148:151], v[88:91]\n" \
"ds_read_b64_tr_b16 v[120:121], v230 offset:16384\n" \
"ds_read_b64_tr_b16 v[122:123], v230 offset:20480\n" \
"v_mfma_f32_16x16x32_bf16 v[28:31], v[124:127], v[140:143], v[28:31]\n" \
"s_add_u32 s59, s59, 1024\n" \
"v_mfma_f32_16x16x32_bf16 v[60:63], v[124:127], v[144:147], v[60:63]\n" \
"v_mfma_f32_16x16x32_bf16 v[92:95], v[124:127], v[148:151], v[92:95]\n" \
"ds_read_b64_tr_b16 v[124:125], v231 offset:16384\n" \
"ds_read_b64_tr_b16 v[126:127], v231 offset:20480\n" \
"s_waitcnt vmcnt(10) lgkmcnt(8)\n" \
"s_barrier\n" \
"v_mfma_f32_16x16x32_bf16 v[0:3], v[96:99], v[152:155], v[0:3]\n" \
"s_add_u32 m0, s56, 0\n" \
"v_mfma_f32_16x16x32_bf16 v[32:35], v[96:99], v[156:159], v[32:35]\n" \
"buffer_load_dwordx4 v234, s[40:43], s58 offen lds\n" \
"v_mfma_f32_16x16x32_bf16 v[64:67], v[96:99], v[160:163], v[64:67]\n" \
"ds_read_b64_tr_b16 v[96:97], v224 offset:24576\n" \
"ds_read_b64_tr_b16 v[98:99], v224 offset:28672\n" \
"v_mfma_f32_16x16x32_bf16 v[4:7], v[100:103], v[152:155], v[4:7]\n" \
"v_mfma_f32_16x16x32_bf16 v[36:39], v[100:103], v[156:159], v[36:39]\n" \
"v_mfma_f32_16x16x32_bf16 v[68:71], v[100:103], v[160:163], v[68:71]\n" \
"ds_read_b64_tr_b16 v[100:101], v225 offset:24576\n" \
"ds_read_b64_tr_b16 v[102:103], v225 offset:28672\n" \
"v_mfma_f32_16x16x32_bf16 v[8:11], v[104:107], v[152:155], v[8:11]\n" \
"buffer_load_dwordx4 v235, s[40:43], s58 offen offset:1024 lds\n" \
"v_mfma_f32_16x16x32_bf16 v[40:43], v[104:107], v[156:159], v[40:43]\n" \
"s_add_u32 s58, s58, s61\n" \
"v_mfma_f32_16x16x32_bf16 v[72:75], v[104:107], v[160:163], v[72:75]\n" \
"ds_read_b64_tr_b16 v[104:105], v226 offset:24576\n" \
"ds_read_b64_tr_b16 v[106:107], v226 offset:28672\n" \
"v_mfma_f32_16x16x32_bf16 v[12:15], v[108:111], v[152:155], v[12:15]\n" \
"v_mfma_f32_16x16x32_bf16 v[44:47], v[108:111], v[156:159], v[44:47]\n" \
"buffer_load_dwordx4 v[188:191], v232, s[44:47], s59 offen sc1\n" \
"v_mfma_f32_16x16x32_bf16 v[76:79], v[108:111], v[160:163], v[76:79]\n" \
"ds_read_b64_tr_b16 v[108:109], v227 offset:24576\n" \
"ds_read_b64_tr_b16 v[110:111], v227 offset:28672\n" \
"s_waitcnt lgkmcnt(8)\n" \
"v_mfma_f32_16x16x32_bf16 v[16:19], v[112:115], v[152:155], v[16:19]\n" \
"v_mfma_f32_16x16x32_bf16 v[48:51], v[112:115], v[156:159], v[48:51]\n" \
"v_mfma_f32_16x16x32_bf16 v[80:83], v[112:115], v[160:163], v[80:83]\n" \
"ds_read_b64_tr_b16 v[112:113], v228 offset:24576\n" \
"ds_read_b64_tr_b16 v[114:115], v228 offset:28672\n" \
"v_mfma_f32_16x16x32_bf16 v[20:23], v[116:119], v[152:155], v[20:23]\n" \
"buffer_load_dwordx4 v[192:195], v232, s[48:51], s59 offen sc1\n" \
"v_mfma_f32_16x16x32_bf16 v[52:55], v[116:119], v[156:159], v[52:55]\n" \
"v_mfma_f32_16x16x32_bf16 v[84:87], v[116:119], v[160:163], v[84:87]\n" \
"ds_read_b64_tr_b16 v[116:117], v229 offset:24576\n" \
"ds_read_b64_tr_b16 v[118:119], v229 offset:28672\n" \
"v_mfma_f32_16x16x32_bf16 v[24:27], v[120:123], v[152:155], v[24:27]\n" \
"v_mfma_f32_16x16x32_bf16 v[56:59], v[120:123], v[156:159], v[56:59]\n" \
"buffer_load_dwordx4 v[196:199], v232, s[52:55], s59 offen sc1\n" \
"v_mfma_f32_16x16x32_bf16 v[88:91], v[120:123], v[160:163], v[88:91]\n" \
"ds_read_b64_tr_b16 v[120:121], v230 offset:24576\n" \
"ds_read_b64_tr_b16 v[122:123], v230 offset:28672\n" \
"v_mfma_f32_16x16x32_bf16 v[28:31], v[124:127], v[152:155], v[28:31]\n" \
"s_add_u32 s59, s59, 1024\n" \
"v_mfma_f32_16x16x32_bf16 v[60:63], v[124:127], v[156:159], v[60:63]\n" \
"v_mfma_f32_16x16x32_bf16 v[92:95], v[124:127], v[160:163], v[92:95]\n" \
"ds_read_b64_tr_b16 v[124:125], v231 offset:24576\n" \
"ds_read_b64_tr_b16 v[126:127], v231 offset:28672\n" \
"s_waitcnt vmcnt(10) lgkmcnt(8)\n" \
"v_mfma_f32_16x16x32_bf16 v[0:3], v[96:99], v[164:167], v[0:3]\n" \
"s_add_u32 m0, s56, 8192\n" \
"v_mfma_f32_16x16x32_bf16 v[32:35], v[96:99], v[168:171], v[32:35]\n" \
"buffer_load_dwordx4 v234, s[40:43], s58 offen lds\n" \
"v_mfma_f32_16x16x32_bf16 v[64:67], v[96:99], v[172:175], v[64:67]\n" \
"ds_read_b64_tr_b16 v[96:97], v224 offset:32768\n" \
"ds_read_b64_tr_b16 v[98:99], v224 offset:36864\n" \
"v_mfma_f32_16x16x32_bf16 v[4:7], v[100:103], v[164:167], v[4:7]\n" \
"v_mfma_f32_16x16x32_bf16 v[36:39], v[100:103], v[168:171], v[36:39]\n" \
"v_mfma_f32_16x16x32_bf16 v[68:71], v[100:103], v[172:175], v[68:71]\n" \
"ds_read_b64_tr_b16 v[100:101], v225 offset:32768\n" \
"ds_read_b64_tr_b16 v[102:103], v225 offset:36864\n" \
"v_mfma_f32_16x16x32_bf16 v[8:11], v[104:107], v[164:167], v[8:11]\n" \
"buffer_load_dwordx4 v235, s[40:43], s58 offen offset:1024 lds\n" \
"v_mfma_f32_16x16x32_bf16 v[40:43], v[104:107], v[168:171], v[40:43]\n" \
"s_add_u32 s58, s58, s61\n" \
"v_mfma_f32_16x16x32_bf16 v[72:75], v[104:107], v[172:175], v[72:75]\n" \
"ds_read_b64_tr_b16 v[104:105], v226 offset:32768\n" \
"ds_read_b64_tr_b16 v[106:107], v226 offset:36864\n" \
"v_mfma_f32_16x16x32_bf16 v[12:15], v[108:111], v[164:167], v[12:15]\n" \
"v_mfma_f32_16x16x32_bf16 v[44:47], v[108:111], v[168:171], v[44:47]\n" \
"buffer_load_dwordx4 v[200:203], v232, s[44:47], s59 offen sc1\n" \
"v_mfma_f32_16x16x32_bf16 v[76:79], v[108:111], v[172:175], v[76:79]\n" \
"ds_read_b64_tr_b16 v[108:109], v227 offset:32768\n" \
"ds_read_b64_tr_b16 v[110:111], v227 offset:36864\n" \
"s_waitcnt lgkmcnt(8)\n" \
"v_mfma_f32_16x16x32_bf16 v[16:19], v[112:115], v[164:167], v[16:19]\n" \
"v_mfma_f32_16x16x32_bf16 v[48:51], v[112:115], v[168:171], v[48:51]\n" \
"v_mfma_f32_16x16x32_bf16 v[80:83], v[112:115], v[172:175], v[80:83]\n" \
"ds_read_b64_tr_b16 v[112:113], v228 offset:32768\n" \
"ds_read_b64_tr_b16 v[114:115], v228 offset:36864\n" \
"v_mfma_f32_16x16x32_bf16 v[20:23], v[116:119], v[164:167], v[20:23]\n" \
"buffer_load_dwordx4 v[204:207], v232, s[48:51], s59 offen sc1\n" \
"v_mfma_f32_16x16x32_bf16 v[52:55], v[116:119], v[168:171], v[52:55]\n" \
"v_mfma_f32_16x16x32_bf16 v[84:87], v[116:119], v[172:175], v[84:87]\n" \
"ds_read_b64_tr_b16 v[116:117], v229 offset:32768\n" \
"ds_read_b64_tr_b16 v[118:119], v229 offset:36864\n" \
"v_mfma_f32_16x16x32_bf16 v[24:27], v[120:123], v[164:167], v[24:27]\n" \
"v_mfma_f32_16x16x32_bf16 v[56:59], v[120:123], v[168:171], v[56:59]\n" \
"buffer_load_dwordx4 v[208:211], v232, s[52:55], s59 offen sc1\n" \
"v_mfma_f32_16x16x32_bf16 v[88:91], v[120:123], v[172:175], v[88:91]\n" \
"ds_read_b64_tr_b16 v[120:121], v230 offset:32768\n" \
"ds_read_b64_tr_b16 v[122:123], v230 offset:36864\n" \
"v_mfma_f32_16x16x32_bf16 v[28:31], v[124:127], v[164:167], v[28:31]\n" \
"s_add_u32 s59, s59, 1024\n" \
"v_mfma_f32_16x16x32_bf16 v[60:63], v[124:127], v[168:171], v[60:63]\n" \
"v_mfma_f32_16x16x32_bf16 v[92:95], v[124:127], v[172:175], v[92:95]\n" \
"ds_read_b64_tr_b16 v[124:125], v231 offset:32768\n" \
"ds_read_b64_tr_b16 v[126:127], v231 offset:36864\n" \
"s_waitcnt vmcnt(10) lgkmcnt(8)\n" \
"s_barrier\n" \
"v_mfma_f32_16x16x32_bf16 v[0:3], v[96:99], v[176:179], v[0:3]\n" \
"s_add_u32 m0, s56, 16384\n" \
"v_mfma_f32_16x16x32_bf16 v[32:35], v[96:99], v[180:183], v[32:35]\n" \
"buffer_load_dwordx4 v234, s[40:43], s58 offen lds\n" \
"v_mfma_f32_16x16x32_bf16 v[64:67], v[96:99], v[184:187], v[64:67]\n" \
"ds_read_b64_tr_b16 v[96:97], v224 offset:40960\n" \
"ds_read_b64_tr_b16 v[98:99], v224 offset:45056\n" \
"v_mfma_f32_16x16x32_bf16 v[4:7], v[100:103], v[176:179], v[4:7]\n" \
"v_mfma_f32_16x16x32_bf16 v[36:39], v[100:103], v[180:183], v[36:39]\n" \
"v_mfma_f32_16x16x32_bf16 v[68:71], v[100:103], v[184:187], v[68:71]\n" \
"ds_read_b64_tr_b16 v[100:101], v225 offset:40960\n" \
"ds_read_b64_tr_b16 v[102:103], v225 offset:45056\n" \
"v_mfma_f32_16x16x32_bf16 v[8:11], v[104:107], v[176:179], v[8:11]\n" \
"buffer_load_dwordx4 v235, s[40:43], s58 offen offset:1024 lds\n" \
"v_mfma_f32_16x16x32_bf16 v[40:43], v[104:107], v[180:183], v[40:43]\n" \
"s_add_u32 s58, s58, s61\n" \
"v_mfma_f32_16x16x32_bf16 v[72:75], v[104:107], v[184:187], v[72:75]\n" \
"ds_read_b64_tr_b16 v[104:105], v226 offset:40960\n" \
"ds_read_b64_tr_b16 v[106:107], v226 offset:45056\n" \
"v_mfma_f32_16x16x32_bf16 v[12:15], v[108:111], v[176:179], v[12:15]\n" \
"v_mfma_f32_16x16x32_bf16 v[44:47], v[108:111], v[180:183], v[44:47]\n" \
"buffer_load_dwordx4 v[212:215], v232, s[44:47], s59 offen sc1\n" \
"v_mfma_f32_16x16x32_bf16 v[76:79], v[108:111], v[184:187], v[76:79]\n" \
"ds_read_b64_tr_b16 v[108:109], v227 offset:40960\n" \
"ds_read_b64_tr_b16 v[110:111], v227 offset:45056\n" \
"s_waitcnt lgkmcnt(8)\n" \
"v_mfma_f32_16x16x32_bf16 v[16:19], v[112:115], v[176:179], v[16:19]\n" \
"v_mfma_f32_16x16x32_bf16 v[48:51], v[112:115], v[180:183], v[48:51]\n" \
"v_mfma_f32_16x16x32_bf16 v[80:83], v[112:115], v[184:187], v[80:83]\n" \
"ds_read_b64_tr_b16 v[112:113], v228 offset:40960\n" \
"ds_read_b64_tr_b16 v[114:115], v228 offset:45056\n" \
"v_mfma_f32_16x16x32_bf16 v[20:23], v[116:119], v[176:179], v[20:23]\n" \
"buffer_load_dwordx4 v[216:219], v232, s[48:51], s59 offen sc1\n" \
"v_mfma_f32_16x16x32_bf16 v[52:55], v[116:119], v[180:183], v[52:55]\n" \
"v_mfma_f32_16x16x32_bf16 v[84:87], v[116:119], v[184:187], v[84:87]\n" \
"ds_read_b64_tr_b16 v[116:117], v229 offset:40960\n" \
"ds_read_b64_tr_b16 v[118:119], v229 offset:45056\n" \
"v_mfma_f32_16x16x32_bf16 v[24:27], v[120:123], v[176:179], v[24:27]\n" \
"v_mfma_f32_16x16x32_bf16 v[56:59], v[120:123], v[180:183], v[56:59]\n" \
"buffer_load_dwordx4 v[220:223], v232, s[52:55], s59 offen sc1\n" \
"v_mfma_f32_16x16x32_bf16 v[88:91], v[120:123], v[184:187], v[88:91]\n" \
"ds_read_b64_tr_b16 v[120:121], v230 offset:40960\n" \
"ds_read_b64_tr_b16 v[122:123], v230 offset:45056\n" \
"v_mfma_f32_16x16x32_bf16 v[28:31], v[124:127], v[176:179], v[28:31]\n" \
"s_add_u32 s59, s59, 1024\n" \
"v_mfma_f32_16x16x32_bf16 v[60:63], v[124:127], v[180:183], v[60:63]\n" \
"v_mfma_f32_16x16x32_bf16 v[92:95], v[124:127], v[184:187], v[92:95]\n" \
"ds_read_b64_tr_b16 v[124:125], v231 offset:40960\n" \
"ds_read_b64_tr_b16 v[126:127], v231 offset:45056\n" \
"s_waitcnt vmcnt(10) lgkmcnt(8)\n" \
"v_mfma_f32_16x16x32_bf16 v[0:3], v[96:99], v[188:191], v[0:3]\n" \
"s_add_u32 m0, s56, 24576\n" \
"v_mfma_f32_16x16x32_bf16 v[32:35], v[96:99], v[192:195], v[32:35]\n" \
"buffer_load_dwordx4 v234, s[40:43], s58 offen lds\n" \
"v_mfma_f32_16x16x32_bf16 v[64:67], v[96:99], v[196:199], v[64:67]\n" \
"ds_read_b64_tr_b16 v[96:97], v224 offset:49152\n" \
"ds_read_b64_tr_b16 v[98:99], v224 offset:53248\n" \
"v_mfma_f32_16x16x32_bf16 v[4:7], v[100:103], v[188:191], v[4:7]\n" \
"v_mfma_f32_16x16x32_bf16 v[36:39], v[100:103], v[192:195], v[36:39]\n" \
"v_mfma_f32_16x16x32_bf16 v[68:71], v[100:103], v[196:199], v[68:71]\n" \
"ds_read_b64_tr_b16 v[100:101], v225 offset:49152\n" \
"ds_read_b64_tr_b16 v[102:103], v225 offset:53248\n" \
"v_mfma_f32_16x16x32_bf16 v[8:11], v[104:107], v[188:191], v[8:11]\n" \
"buffer_load_dwordx4 v235, s[40:43], s58 offen offset:1024 lds\n" \
"v_mfma_f32_16x16x32_bf16 v[40:43], v[104:107], v[192:195], v[40:43]\n" \
"s_add_u32 s58, s58, s61\n" \
"v_mfma_f32_16x16x32_bf16 v[72:75], v[104:107], v[196:199], v[72:75]\n" \
"ds_read_b64_tr_b16 v[104:105], v226 offset:49152\n" \
"ds_read_b64_tr_b16 v[106:107], v226 offset:53248\n" \
"v_mfma_f32_16x16x32_bf16 v[12:15], v[108:111], v[188:191], v[12:15]\n" \
"v_mfma_f32_16x16x32_bf16 v[44:47], v[108:111], v[192:195], v[44:47]\n" \
"buffer_load_dwordx4 v[128:131], v232, s[44:47], s59 offen sc1\n" \
"v_mfma_f32_16x16x32_bf16 v[76:79], v[108:111], v[196:199], v[76:79]\n" \
"ds_read_b64_tr_b16 v[108:109], v227 offset:49152\n" \
"ds_read_b64_tr_b16 v[110:111], v227 offset:53248\n" \
"s_waitcnt lgkmcnt(8)\n" \
"v_mfma_f32_16x16x32_bf16 v[16:19], v[112:115], v[188:191], v[16:19]\n" \
"v_mfma_f32_16x16x32_bf16 v[48:51], v[112:115], v[192:195], v[48:51]\n" \
"v_mfma_f32_16x16x32_bf16 v[80:83], v[112:115], v[196:199], v[80:83]\n" \
"ds_read_b64_tr_b16 v[112:113], v228 offset:49152\n" \
"ds_read_b64_tr_b16 v[114:115], v228 offset:53248\n" \
"v_mfma_f32_16x16x32_bf16 v[20:23], v[116:119], v[188:191], v[20:23]\n" \
"buffer_load_dwordx4 v[132:135], v232, s[48:51], s59 offen sc1\n" \
"v_mfma_f32_16x16x32_bf16 v[52:55], v[116:119], v[192:195], v[52:55]\n" \
"v_mfma_f32_16x16x32_bf16 v[84:87], v[116:119], v[196:199], v[84:87]\n" \
"ds_read_b64_tr_b16 v[116:117], v229 offset:49152\n" \
"ds_read_b64_tr_b16 v[118:119], v229 offset:53248\n" \
"v_mfma_f32_16x16x32_bf16 v[24:27], v[120:123], v[188:191], v[24:27]\n" \
"v_mfma_f32_16x16x32_bf16 v[56:59], v[120:123], v[192:195], v[56:59]\n" \
"buffer_load_dwordx4 v[136:139], v232, s[52:55], s59 offen sc1\n" \
"v_mfma_f32_16x16x32_bf16 v[88:91], v[120:123], v[196:199], v[88:91]\n" \
"ds_read_b64_tr_b16 v[120:121], v230 offset:49152\n" \
"ds_read_b64_tr_b16 v[122:123], v230 offset:53248\n" \
"v_mfma_f32_16x16x32_bf16 v[28:31], v[124:127], v[188:191], v[28:31]\n" \
"s_add_u32 s59, s59, 1024\n" \
"v_mfma_f32_16x16x32_bf16 v[60:63], v[124:127], v[192:195], v[60:63]\n" \
"v_mfma_f32_16x16x32_bf16 v[92:95], v[124:127], v[196:199], v[92:95]\n" \
"ds_read_b64_tr_b16 v[124:125], v231 offset:49152\n" \
"ds_read_b64_tr_b16 v[126:127], v231 offset:53248\n" \
"s_waitcnt vmcnt(10) lgkmcnt(8)\n" \
"s_barrier\n" \
"v_mfma_f32_16x16x32_bf16 v[0:3], v[96:99], v[200:203], v[0:3]\n" \
"s_add_u32 m0, s56, 32768\n" \
"v_mfma_f32_16x16x32_bf16 v[32:35], v[96:99], v[204:207], v[32:35]\n" \
"buffer_load_dwordx4 v234, s[40:43], s58 offen lds\n" \
"v_mfma_f32_16x16x32_bf16 v[64:67], v[96:99], v[208:211], v[64:67]\n" \
"ds_read_b64_tr_b16 v[96:97], v224 offset:57344\n" \
"ds_read_b64_tr_b16 v[98:99], v224 offset:61440\n" \
"v_mfma_f32_16x16x32_bf16 v[4:7], v[100:103], v[200:203], v[4:7]\n" \
"v_mfma_f32_16x16x32_bf16 v[36:39], v[100:103], v[204:207], v[36:39]\n" \
"v_mfma_f32_16x16x32_bf16 v[68:71], v[100:103], v[208:211], v[68:71]\n" \
"ds_read_b64_tr_b16 v[100:101], v225 offset:57344\n" \
"ds_read_b64_tr_b16 v[102:103], v225 offset:61440\n" \
"v_mfma_f32_16x16x32_bf16 v[8:11], v[104:107], v[200:203], v[8:11]\n" \
"buffer_load_dwordx4 v235, s[40:43], s58 offen offset:1024 lds\n" \
"v_mfma_f32_16x16x32_bf16 v[40:43], v[104:107], v[204:207], v[40:43]\n" \
"s_add_u32 s58, s58, s61\n" \
"v_mfma_f32_16x16x32_bf16 v[72:75], v[104:107], v[208:211], v[72:75]\n" \
"ds_read_b64_tr_b16 v[104:105], v226 offset:57344\n" \
"ds_read_b64_tr_b16 v[106:107], v226 offset:61440\n" \
"v_mfma_f32_16x16x32_bf16 v[12:15], v[108:111], v[200:203], v[12:15]\n" \
"v_mfma_f32_16x16x32_bf16 v[44:47], v[108:111], v[204:207], v[44:47]\n" \
"buffer_load_dwordx4 v[140:143], v232, s[44:47], s59 offen sc1\n" \
"v_mfma_f32_16x16x32_bf16 v[76:79], v[108:111], v[208:211], v[76:79]\n" \
"ds_read_b64_tr_b16 v[108:109], v227 offset:57344\n" \
"ds_read_b64_tr_b16 v[110:111], v227 offset:61440\n" \
"s_waitcnt lgkmcnt(8)\n" \
"v_mfma_f32_16x16x32_bf16 v[16:19], v[112:115], v[200:203], v[16:19]\n" \
"v_mfma_f32_16x16x32_bf16 v[48:51], v[112:115], v[204:207], v[48:51]\n" \
"v_mfma_f32_16x16x32_bf16 v[80:83], v[112:115], v[208:211], v[80:83]\n" \
"ds_read_b64_tr_b16 v[112:113], v228 offset:57344\n" \
"ds_read_b64_tr_b16 v[114:115], v228 offset:61440\n" \
"v_mfma_f32_16x16x32_bf16 v[20:23], v[116:119], v[200:203], v[20:23]\n" \
"buffer_load_dwordx4 v[144:147], v232, s[48:51], s59 offen sc1\n" \
"v_mfma_f32_16x16x32_bf16 v[52:55], v[116:119], v[204:207], v[52:55]\n" \
"v_mfma_f32_16x16x32_bf16 v[84:87], v[116:119], v[208:211], v[84:87]\n" \
"ds_read_b64_tr_b16 v[116:117], v229 offset:57344\n" \
"ds_read_b64_tr_b16 v[118:119], v229 offset:61440\n" \
"v_mfma_f32_16x16x32_bf16 v[24:27], v[120:123], v[200:203], v[24:27]\n" \
"v_mfma_f32_16x16x32_bf16 v[56:59], v[120:123], v[204:207], v[56:59]\n" \
"buffer_load_dwordx4 v[148:151], v232, s[52:55], s59 offen sc1\n" \
"v_mfma_f32_16x16x32_bf16 v[88:91], v[120:123], v[208:211], v[88:91]\n" \
"ds_read_b64_tr_b16 v[120:121], v230 offset:57344\n" \
"ds_read_b64_tr_b16 v[122:123], v230 offset:61440\n" \
"v_mfma_f32_16x16x32_bf16 v[28:31], v[124:127], v[200:203], v[28:31]\n" \
"s_add_u32 s59, s59, 1024\n" \
"v_mfma_f32_16x16x32_bf16 v[60:63], v[124:127], v[204:207], v[60:63]\n" \
"v_mfma_f32_16x16x32_bf16 v[92:95], v[124:127], v[208:211], v[92:95]\n" \
"ds_read_b64_tr_b16 v[124:125], v231 offset:57344\n" \
"ds_read_b64_tr_b16 v[126:127], v231 offset:61440\n" \
"s_waitcnt vmcnt(10) lgkmcnt(8)\n" \
"v_mfma_f32_16x16x32_bf16 v[0:3], v[96:99], v[212:215], v[0:3]\n" \
"s_add_u32 m0, s56, 40960\n" \
"v_mfma_f32_16x16x32_bf16 v[32:35], v[96:99], v[216:219], v[32:35]\n" \
"buffer_load_dwordx4 v234, s[40:43], s58 offen lds\n" \
"v_mfma_f32_16x16x32_bf16 v[64:67], v[96:99], v[220:223], v[64:67]\n" \
"ds_read_b64_tr_b16 v[96:97], v224 offset:0\n" \
"ds_read_b64_tr_b16 v[98:99], v224 offset:4096\n" \
"v_mfma_f32_16x16x32_bf16 v[4:7], v[100:103], v[212:215], v[4:7]\n" \
"v_mfma_f32_16x16x32_bf16 v[36:39], v[100:103], v[216:219], v[36:39]\n" \
"v_mfma_f32_16x16x32_bf16 v[68:71], v[100:103], v[220:223], v[68:71]\n" \
"ds_read_b64_tr_b16 v[100:101], v225 offset:0\n" \
"ds_read_b64_tr_b16 v[102:103], v225 offset:4096\n" \
"v_mfma_f32_16x16x32_bf16 v[8:11], v[104:107], v[212:215], v[8:11]\n" \
"buffer_load_dwordx4 v235, s[40:43], s58 offen offset:1024 lds\n" \
"v_mfma_f32_16x16x32_bf16 v[40:43], v[104:107], v[216:219], v[40:43]\n" \
"s_add_u32 s58, s58, s61\n" \
"v_mfma_f32_16x16x32_bf16 v[72:75], v[104:107], v[220:223], v[72:75]\n" \
"ds_read_b64_tr_b16 v[104:105], v226 offset:0\n" \
"ds_read_b64_tr_b16 v[106:107], v226 offset:4096\n" \
"v_mfma_f32_16x16x32_bf16 v[12:15], v[108:111], v[212:215], v[12:15]\n" \
"v_mfma_f32_16x16x32_bf16 v[44:47], v[108:111], v[216:219], v[44:47]\n" \
"buffer_load_dwordx4 v[152:155], v232, s[44:47], s59 offen sc1\n" \
"v_mfma_f32_16x16x32_bf16 v[76:79], v[108:111], v[220:223], v[76:79]\n" \
"ds_read_b64_tr_b16 v[108:109], v227 offset:0\n" \
"ds_read_b64_tr_b16 v[110:111], v227 offset:4096\n" \
"s_waitcnt lgkmcnt(8)\n" \
"v_mfma_f32_16x16x32_bf16 v[16:19], v[112:115], v[212:215], v[16:19]\n" \
"v_mfma_f32_16x16x32_bf16 v[48:51], v[112:115], v[216:219], v[48:51]\n" \
"v_mfma_f32_16x16x32_bf16 v[80:83], v[112:115], v[220:223], v[80:83]\n" \
"ds_read_b64_tr_b16 v[112:113], v228 offset:0\n" \
"ds_read_b64_tr_b16 v[114:115], v228 offset:4096\n" \
"v_mfma_f32_16x16x32_bf16 v[20:23], v[116:119], v[212:215], v[20:23]\n" \
"buffer_load_dwordx4 v[156:159], v232, s[48:51], s59 offen sc1\n" \
"v_mfma_f32_16x16x32_bf16 v[52:55], v[116:119], v[216:219], v[52:55]\n" \
"v_mfma_f32_16x16x32_bf16 v[84:87], v[116:119], v[220:223], v[84:87]\n" \
"ds_read_b64_tr_b16 v[116:117], v229 offset:0\n" \
"ds_read_b64_tr_b16 v[118:119], v229 offset:4096\n" \
"v_mfma_f32_16x16x32_bf16 v[24:27], v[120:123], v[212:215], v[24:27]\n" \
"v_mfma_f32_16x16x32_bf16 v[56:59], v[120:123], v[216:219], v[56:59]\n" \
"buffer_load_dwordx4 v[160:163], v232, s[52:55], s59 offen sc1\n" \
"v_mfma_f32_16x16x32_bf16 v[88:91], v[120:123], v[220:223], v[88:91]\n" \
"ds_read_b64_tr_b16 v[120:121], v230 offset:0\n" \
"ds_read_b64_tr_b16 v[122:123], v230 offset:4096\n" \
"v_mfma_f32_16x16x32_bf16 v[28:31], v[124:127], v[212:215], v[28:31]\n" \
"s_add_u32 s59, s59, 1024\n" \
"s_sub_u32 s60, s60, 1\n" \
"v_mfma_f32_16x16x32_bf16 v[60:63], v[124:127], v[216:219], v[60:63]\n" \
"s_cmp_eq_u32 s60, 0\n" \
"v_mfma_f32_16x16x32_bf16 v[92:95], v[124:127], v[220:223], v[92:95]\n" \
"ds_read_b64_tr_b16 v[124:125], v231 offset:0\n" \
"ds_read_b64_tr_b16 v[126:127], v231 offset:4096\n" \
"s_cbranch_scc0 Lg1_%=_loop\n" \
"s_waitcnt vmcnt(10) lgkmcnt(8)\n" \
"s_barrier\n" \
"v_mfma_f32_16x16x32_bf16 v[0:3], v[96:99], v[128:131], v[0:3]\n" \
"s_add_u32 m0, s56, 49152\n" \
"v_mfma_f32_16x16x32_bf16 v[32:35], v[96:99], v[132:135], v[32:35]\n" \
"buffer_load_dwordx4 v234, s[40:43], s58 offen lds\n" \
"v_mfma_f32_16x16x32_bf16 v[64:67], v[96:99], v[136:139], v[64:67]\n" \
"ds_read_b64_tr_b16 v[96:97], v224 offset:8192\n" \
"ds_read_b64_tr_b16 v[98:99], v224 offset:12288\n" \
"v_mfma_f32_16x16x32_bf16 v[4:7], v[100:103], v[128:131], v[4:7]\n" \
"v_mfma_f32_16x16x32_bf16 v[36:39], v[100:103], v[132:135], v[36:39]\n" \
"v_mfma_f32_16x16x32_bf16 v[68:71], v[100:103], v[136:139], v[68:71]\n" \
"ds_read_b64_tr_b16 v[100:101], v225 offset:8192\n" \
"ds_read_b64_tr_b16 v[102:103], v225 offset:12288\n" \
"v_mfma_f32_16x16x32_bf16 v[8:11], v[104:107], v[128:131], v[8:11]\n" \
"buffer_load_dwordx4 v235, s[40:43], s58 offen offset:1024 lds\n" \
"v_mfma_f32_16x16x32_bf16 v[40:43], v[104:107], v[132:135], v[40:43]\n" \
"s_add_u32 s58, s58, s61\n" \
"v_mfma_f32_16x16x32_bf16 v[72:75], v[104:107], v[136:139], v[72:75]\n" \
"ds_read_b64_tr_b16 v[104:105], v226 offset:8192\n" \
"ds_read_b64_tr_b16 v[106:107], v226 offset:12288\n" \
"v_mfma_f32_16x16x32_bf16 v[12:15], v[108:111], v[128:131], v[12:15]\n" \
"buffer_load_dwordx4 v[164:167], v232, s[44:47], s59 offen sc1\n" \
"v_mfma_f32_16x16x32_bf16 v[44:47], v[108:111], v[132:135], v[44:47]\n" \
"v_mfma_f32_16x16x32_bf16 v[76:79], v[108:111], v[136:139], v[76:79]\n" \
"ds_read_b64_tr_b16 v[108:109], v227 offset:8192\n" \
"ds_read_b64_tr_b16 v[110:111], v227 offset:12288\n" \
"s_waitcnt lgkmcnt(8)\n" \
"v_mfma_f32_16x16x32_bf16 v[16:19], v[112:115], v[128:131], v[16:19]\n" \
"buffer_load_dwordx4 v[168:171], v232, s[48:51], s59 offen sc1\n" \
"v_mfma_f32_16x16x32_bf16 v[48:51], v[112:115], v[132:135], v[48:51]\n" \
"v_mfma_f32_16x16x32_bf16 v[80:83], v[112:115], v[136:139], v[80:83]\n" \
"ds_read_b64_tr_b16 v[112:113], v228 offset:8192\n" \
"ds_read_b64_tr_b16 v[114:115], v228 offset:12288\n" \
"v_mfma_f32_16x16x32_bf16 v[20:23], v[116:119], v[128:131], v[20:23]\n" \
"buffer_load_dwordx4 v[172:175], v232, s[52:55], s59 offen sc1\n" \
"v_mfma_f32_16x16x32_bf16 v[52:55], v[116:119], v[132:135], v[52:55]\n" \
"v_mfma_f32_16x16x32_bf16 v[84:87], v[116:119], v[136:139], v[84:87]\n" \
"ds_read_b64_tr_b16 v[116:117], v229 offset:8192\n" \
"ds_read_b64_tr_b16 v[118:119], v229 offset:12288\n" \
"v_mfma_f32_16x16x32_bf16 v[24:27], v[120:123], v[128:131], v[24:27]\n" \
"buffer_load_dwordx4 v[176:179], v232, s[44:47], s59 offen offset:1024 sc1\n" \
"v_mfma_f32_16x16x32_bf16 v[56:59], v[120:123], v[132:135], v[56:59]\n" \
"v_mfma_f32_16x16x32_bf16 v[88:91], v[120:123], v[136:139], v[88:91]\n" \
"ds_read_b64_tr_b16 v[120:121], v230 offset:8192\n" \
"ds_read_b64_tr_b16 v[122:123], v230 offset:12288\n" \
"v_mfma_f32_16x16x32_bf16 v[28:31], v[124:127], v[128:131], v[28:31]\n" \
"buffer_load_dwordx4 v[180:183], v232, s[48:51], s59 offen offset:1024 sc1\n" \
"v_mfma_f32_16x16x32_bf16 v[60:63], v[124:127], v[132:135], v[60:63]\n" \
"buffer_load_dwordx4 v[184:187], v232, s[52:55], s59 offen offset:1024 sc1\n" \
"v_mfma_f32_16x16x32_bf16 v[92:95], v[124:127], v[136:139], v[92:95]\n" \
"ds_read_b64_tr_b16 v[124:125], v231 offset:8192\n" \
"ds_read_b64_tr_b16 v[126:127], v231 offset:12288\n" \
"s_waitcnt vmcnt(13) lgkmcnt(8)\n" \
"v_mfma_f32_16x16x32_bf16 v[0:3], v[96:99], v[140:143], v[0:3]\n" \
"s_add_u32 m0, s56, 57344\n" \
"v_mfma_f32_16x16x32_bf16 v[32:35], v[96:99], v[144:147], v[32:35]\n" \
"buffer_load_dwordx4 v234, s[40:43], s58 offen lds\n" \
"v_mfma_f32_16x16x32_bf16 v[64:67], v[96:99], v[148:151], v[64:67]\n" \
"ds_read_b64_tr_b16 v[96:97], v224 offset:16384\n" \
"ds_read_b64_tr_b16 v[98:99], v224 offset:20480\n" \
"v_mfma_f32_16x16x32_bf16 v[4:7], v[100:103], v[140:143], v[4:7]\n" \
"v_mfma_f32_16x16x32_bf16 v[36:39], v[100:103], v[144:147], v[36:39]\n" \
"v_mfma_f32_16x16x32_bf16 v[68:71], v[100:103], v[148:151], v[68:71]\n" \
"ds_read_b64_tr_b16 v[100:101], v225 offset:16384\n" \
"ds_read_b64_tr_b16 v[102:103], v225 offset:20480\n" \
"v_mfma_f32_16x16x32_bf16 v[8:11], v[104:107], v[140:143], v[8:11]\n" \
"buffer_load_dwordx4 v235, s[40:43], s58 offen offset:1024 lds\n" \
"v_mfma_f32_16x16x32_bf16 v[40:43], v[104:107], v[144:147], v[40:43]\n" \
"s_add_u32 s58, s58, s61\n" \
"v_mfma_f32_16x16x32_bf16 v[72:75], v[104:107], v[148:151], v[72:75]\n" \
"ds_read_b64_tr_b16 v[104:105], v226 offset:16384\n" \
"ds_read_b64_tr_b16 v[106:107], v226 offset:20480\n" \
"v_mfma_f32_16x16x32_bf16 v[12:15], v[108:111], v[140:143], v[12:15]\n" \
"buffer_load_dwordx4 v[188:191], v232, s[44:47], s59 offen offset:2048 sc1\n" \
"v_mfma_f32_16x16x32_bf16 v[44:47], v[108:111], v[144:147], v[44:47]\n" \
"v_mfma_f32_16x16x32_bf16 v[76:79], v[108:111], v[148:151], v[76:79]\n" \
"ds_read_b64_tr_b16 v[108:109], v227 offset:16384\n" \
"ds_read_b64_tr_b16 v[110:111], v227 offset:20480\n" \
"s_waitcnt lgkmcnt(8)\n" \
"v_mfma_f32_16x16x32_bf16 v[16:19], v[112:115], v[140:143], v[16:19]\n" \
"buffer_load_dwordx4 v[192:195], v232, s[48:51], s59 offen offset:2048 sc1\n" \
"v_mfma_f32_16x16x32_bf16 v[48:51], v[112:115], v[144:147], v[48:51]\n" \
"v_mfma_f32_16x16x32_bf16 v[80:83], v[112:115], v[148:151], v[80:83]\n" \
"ds_read_b64_tr_b16 v[112:113], v224 offset:24576\n" \
"ds_read_b64_tr_b16 v[114:115], v224 offset:28672\n" \
"v_mfma_f32_16x16x32_bf16 v[20:23], v[116:119], v[140:143], v[20:23]\n" \
"buffer_load_dwordx4 v[196:199], v232, s[52:55], s59 offen offset:2048 sc1\n" \
"v_mfma_f32_16x16x32_bf16 v[52:55], v[116:119], v[144:147], v[52:55]\n" \
"v_mfma_f32_16x16x32_bf16 v[84:87], v[116:119], v[148:151], v[84:87]\n" \
"ds_read_b64_tr_b16 v[116:117], v225 offset:24576\n" \
"ds_read_b64_tr_b16 v[118:119], v225 offset:28672\n" \
"v_mfma_f32_16x16x32_bf16 v[24:27], v[120:123], v[140:143], v[24:27]\n" \
"buffer_load_dwordx4 v[200:203], v232, s[44:47], s59 offen offset:3072 sc1\n" \
"v_mfma_f32_16x16x32_bf16 v[56:59], v[120:123], v[144:147], v[56:59]\n" \
"v_mfma_f32_16x16x32_bf16 v[88:91], v[120:123], v[148:151], v[88:91]\n" \
"ds_read_b64_tr_b16 v[120:121], v226 offset:24576\n" \
"ds_read_b64_tr_b16 v[122:123], v226 offset:28672\n" \
"v_mfma_f32_16x16x32_bf16 v[28:31], v[124:127], v[140:143], v[28:31]\n" \
"buffer_load_dwordx4 v[204:207], v232, s[48:51], s59 offen offset:3072 sc1\n" \
"v_mfma_f32_16x16x32_bf16 v[60:63], v[124:127], v[144:147], v[60:63]\n" \
"buffer_load_dwordx4 v[208:211], v232, s[52:55], s59 offen offset:3072 sc1\n" \
"v_mfma_f32_16x16x32_bf16 v[92:95], v[124:127], v[148:151], v[92:95]\n" \
"ds_read_b64_tr_b16 v[124:125], v227 offset:24576\n" \
"ds_read_b64_tr_b16 v[126:127], v227 offset:28672\n" \
"s_waitcnt vmcnt(16)\n" \
"s_barrier\n" \
"s_add_u32 s59, s59, 4096\n" \
"s_waitcnt lgkmcnt(8)\n" \
"v_mfma_f32_16x16x32_bf16 v[0:3], v[96:99], v[152:155], v[0:3]\n" \
"buffer_load_dwordx4 v[212:215], v232, s[44:47], s59 offen sc1\n" \
"v_mfma_f32_16x16x32_bf16 v[32:35], v[96:99], v[156:159], v[32:35]\n" \
"v_mfma_f32_16x16x32_bf16 v[64:67], v[96:99], v[160:163], v[64:67]\n" \
"ds_read_b64_tr_b16 v[96:97], v224 offset:32768\n" \
"ds_read_b64_tr_b16 v[98:99], v224 offset:36864\n" \
"v_mfma_f32_16x16x32_bf16 v[4:7], v[100:103], v[152:155], v[4:7]\n" \
"buffer_load_dwordx4 v[216:219], v232, s[48:51], s59 offen sc1\n" \
"v_mfma_f32_16x16x32_bf16 v[36:39], v[100:103], v[156:159], v[36:39]\n" \
"v_mfma_f32_16x16x32_bf16 v[68:71], v[100:103], v[160:163], v[68:71]\n" \
"ds_read_b64_tr_b16 v[100:101], v225 offset:32768\n" \
"ds_read_b64_tr_b16 v[102:103], v225 offset:36864\n" \
"v_mfma_f32_16x16x32_bf16 v[8:11], v[104:107], v[152:155], v[8:11]\n" \
"buffer_load_dwordx4 v[220:223], v232, s[52:55], s59 offen sc1\n" \
"v_mfma_f32_16x16x32_bf16 v[40:43], v[104:107], v[156:159], v[40:43]\n" \
"v_mfma_f32_16x16x32_bf16 v[72:75], v[104:107], v[160:163], v[72:75]\n" \
"ds_read_b64_tr_b16 v[104:105], v226 offset:32768\n" \
"ds_read_b64_tr_b16 v[106:107], v226 offset:36864\n" \
"v_mfma_f32_16x16x32_bf16 v[12:15], v[108:111], v[152:155], v[12:15]\n" \
"v_mfma_f32_16x16x32_bf16 v[44:47], v[108:111], v[156:159], v[44:47]\n" \
"s_add_u32 m0, s69, 24576\n" \
"s_mov_b32 s60, 0\n" \
"v_mfma_f32_16x16x32_bf16 v[76:79], v[108:111], v[160:163], v[76:79]\n" \
"ds_read_b64_tr_b16 v[108:109], v227 offset:32768\n" \
"ds_read_b64_tr_b16 v[110:111], v227 offset:36864\n" \
"s_waitcnt vmcnt(14)\n" \
"s_waitcnt lgkmcnt(8)\n" \
"v_mfma_f32_16x16x32_bf16 v[0:3], v[112:115], v[164:167], v[0:3]\n" \
"buffer_load_dwordx4 v236, s[72:75], s60 offen lds\n" \
"v_mfma_f32_16x16x32_bf16 v[32:35], v[112:115], v[168:171], v[32:35]\n" \
"v_mfma_f32_16x16x32_bf16 v[64:67], v[112:115], v[172:175], v[64:67]\n" \
"ds_read_b64_tr_b16 v[112:113], v224 offset:40960\n" \
"ds_read_b64_tr_b16 v[114:115], v224 offset:45056\n" \
"v_mfma_f32_16x16x32_bf16 v[4:7], v[116:119], v[164:167], v[4:7]\n" \
"buffer_load_dwordx4 v237, s[72:75], s60 offen offset:1024 lds\n" \
"v_mfma_f32_16x16x32_bf16 v[36:39], v[116:119], v[168:171], v[36:39]\n" \
"s_add_u32 m0, s69, 32768\n" \
"s_mov_b32 s60, 131072\n" \
"v_mfma_f32_16x16x32_bf16 v[68:71], v[116:119], v[172:175], v[68:71]\n" \
"ds_read_b64_tr_b16 v[116:117], v225 offset:40960\n" \
"ds_read_b64_tr_b16 v[118:119], v225 offset:45056\n" \
"v_mfma_f32_16x16x32_bf16 v[8:11], v[120:123], v[164:167], v[8:11]\n" \
"buffer_load_dwordx4 v236, s[72:75], s60 offen lds\n" \
"v_mfma_f32_16x16x32_bf16 v[40:43], v[120:123], v[168:171], v[40:43]\n" \
"v_mfma_f32_16x16x32_bf16 v[72:75], v[120:123], v[172:175], v[72:75]\n" \
"ds_read_b64_tr_b16 v[120:121], v226 offset:40960\n" \
"ds_read_b64_tr_b16 v[122:123], v226 offset:45056\n" \
"v_mfma_f32_16x16x32_bf16 v[12:15], v[124:127], v[164:167], v[12:15]\n" \
"buffer_load_dwordx4 v237, s[72:75], s60 offen offset:1024 lds\n" \
"v_mfma_f32_16x16x32_bf16 v[44:47], v[124:127], v[168:171], v[44:47]\n" \
"s_add_u32 m0, s69, 40960\n" \
"s_mov_b32 s60, 262144\n" \
"v_mfma_f32_16x16x32_bf16 v[76:79], v[124:127], v[172:175], v[76:79]\n" \
"ds_read_b64_tr_b16 v[124:125], v227 offset:40960\n" \
"ds_read_b64_tr_b16 v[126:127], v227 offset:45056\n" \
"s_waitcnt vmcnt(13)\n" \
"s_barrier\n" \
"s_waitcnt lgkmcnt(8)\n" \
"v_mfma_f32_16x16x32_bf16 v[0:3], v[96:99], v[176:179], v[0:3]\n" \
"buffer_load_dwordx4 v236, s[72:75], s60 offen lds\n" \
"v_mfma_f32_16x16x32_bf16 v[32:35], v[96:99], v[180:183], v[32:35]\n" \
"v_mfma_f32_16x16x32_bf16 v[64:67], v[96:99], v[184:187], v[64:67]\n" \
"ds_read_b64_tr_b16 v[96:97], v224 offset:49152\n" \
"ds_read_b64_tr_b16 v[98:99], v224 offset:53248\n" \
"v_mfma_f32_16x16x32_bf16 v[4:7], v[100:103], v[176:179], v[4:7]\n" \
"buffer_load_dwordx4 v237, s[72:75], s60 offen offset:1024 lds\n" \
"v_mfma_f32_16x16x32_bf16 v[36:39], v[100:103], v[180:183], v[36:39]\n" \
"s_add_u32 m0, s69, 49152\n" \
"s_mov_b32 s60, 393216\n" \
"v_mfma_f32_16x16x32_bf16 v[68:71], v[100:103], v[184:187], v[68:71]\n" \
"ds_read_b64_tr_b16 v[100:101], v225 offset:49152\n" \
"ds_read_b64_tr_b16 v[102:103], v225 offset:53248\n" \
"v_mfma_f32_16x16x32_bf16 v[8:11], v[104:107], v[176:179], v[8:11]\n" \
"buffer_load_dwordx4 v236, s[72:75], s60 offen lds\n" \
"v_mfma_f32_16x16x32_bf16 v[40:43], v[104:107], v[180:183], v[40:43]\n" \
"v_mfma_f32_16x16x32_bf16 v[72:75], v[104:107], v[184:187], v[72:75]\n" \
"ds_read_b64_tr_b16 v[104:105], v226 offset:49152\n" \
"ds_read_b64_tr_b16 v[106:107], v226 offset:53248\n" \
"v_mfma_f32_16x16x32_bf16 v[12:15], v[108:111], v[176:179], v[12:15]\n" \
"buffer_load_dwordx4 v237, s[72:75], s60 offen offset:1024 lds\n" \
"v_mfma_f32_16x16x32_bf16 v[44:47], v[108:111], v[180:183], v[44:47]\n" \
"s_add_u32 m0, s69, 57344\n" \
"s_mov_b32 s60, 524288\n" \
"v_mfma_f32_16x16x32_bf16 v[76:79], v[108:111], v[184:187], v[76:79]\n" \
"ds_read_b64_tr_b16 v[108:109], v227 offset:49152\n" \
"ds_read_b64_tr_b16 v[110:111], v227 offset:53248\n" \
"s_waitcnt vmcnt(14)\n" \
"s_waitcnt lgkmcnt(8)\n" \
"v_mfma_f32_16x16x32_bf16 v[0:3], v[112:115], v[188:191], v[0:3]\n" \
"buffer_load_dwordx4 v236, s[72:75], s60 offen lds\n" \
"v_mfma_f32_16x16x32_bf16 v[32:35], v[112:115], v[192:195], v[32:35]\n" \
"v_mfma_f32_16x16x32_bf16 v[64:67], v[112:115], v[196:199], v[64:67]\n" \
"ds_read_b64_tr_b16 v[112:113], v224 offset:57344\n" \
"ds_read_b64_tr_b16 v[114:115], v224 offset:61440\n" \
"v_mfma_f32_16x16x32_bf16 v[4:7], v[116:119], v[188:191], v[4:7]\n" \
"buffer_load_dwordx4 v237, s[72:75], s60 offen offset:1024 lds\n" \
"v_mfma_f32_16x16x32_bf16 v[36:39], v[116:119], v[192:195], v[36:39]\n" \
"s_add_u32 m0, s69, 0\n" \
"s_mov_b32 s60, 655360\n" \
"v_mfma_f32_16x16x32_bf16 v[68:71], v[116:119], v[196:199], v[68:71]\n" \
"ds_read_b64_tr_b16 v[116:117], v225 offset:57344\n" \
"ds_read_b64_tr_b16 v[118:119], v225 offset:61440\n" \
"v_mfma_f32_16x16x32_bf16 v[8:11], v[120:123], v[188:191], v[8:11]\n" \
"buffer_load_dwordx4 v236, s[72:75], s60 offen lds\n" \
"v_mfma_f32_16x16x32_bf16 v[40:43], v[120:123], v[192:195], v[40:43]\n" \
"v_mfma_f32_16x16x32_bf16 v[72:75], v[120:123], v[196:199], v[72:75]\n" \
"ds_read_b64_tr_b16 v[120:121], v226 offset:57344\n" \
"ds_read_b64_tr_b16 v[122:123], v226 offset:61440\n" \
"v_mfma_f32_16x16x32_bf16 v[12:15], v[124:127], v[188:191], v[12:15]\n" \
"buffer_load_dwordx4 v237, s[72:75], s60 offen offset:1024 lds\n" \
"v_mfma_f32_16x16x32_bf16 v[44:47], v[124:127], v[192:195], v[44:47]\n" \
"v_mfma_f32_16x16x32_bf16 v[76:79], v[124:127], v[196:199], v[76:79]\n" \
"ds_read_b64_tr_b16 v[124:125], v227 offset:57344\n" \
"ds_read_b64_tr_b16 v[126:127], v227 offset:61440\n" \
"s_waitcnt vmcnt(15)\n" \
"s_waitcnt lgkmcnt(8)\n" \
"v_mfma_f32_16x16x32_bf16 v[0:3], v[96:99], v[200:203], v[0:3]\n" \
"v_mfma_f32_16x16x32_bf16 v[32:35], v[96:99], v[204:207], v[32:35]\n" \
"v_mfma_f32_16x16x32_bf16 v[64:67], v[96:99], v[208:211], v[64:67]\n" \
"ds_read_b64_tr_b16 v[96:97], v228 offset:16384\n" \
"ds_read_b64_tr_b16 v[98:99], v228 offset:20480\n" \
"v_mfma_f32_16x16x32_bf16 v[4:7], v[100:103], v[200:203], v[4:7]\n" \
"v_mfma_f32_16x16x32_bf16 v[36:39], v[100:103], v[204:207], v[36:39]\n" \
"v_mfma_f32_16x16x32_bf16 v[68:71], v[100:103], v[208:211], v[68:71]\n" \
"ds_read_b64_tr_b16 v[100:101], v229 offset:16384\n" \
"ds_read_b64_tr_b16 v[102:103], v229 offset:20480\n" \
"v_mfma_f32_16x16x32_bf16 v[8:11], v[104:107], v[200:203], v[8:11]\n" \
"v_mfma_f32_16x16x32_bf16 v[40:43], v[104:107], v[204:207], v[40:43]\n" \
"v_mfma_f32_16x16x32_bf16 v[72:75], v[104:107], v[208:211], v[72:75]\n" \
"ds_read_b64_tr_b16 v[104:105], v230 offset:16384\n" \
"ds_read_b64_tr_b16 v[106:107], v230 offset:20480\n" \
"v_mfma_f32_16x16x32_bf16 v[12:15], v[108:111], v[200:203], v[12:15]\n" \
"v_mfma_f32_16x16x32_bf16 v[44:47], v[108:111], v[204:207], v[44:47]\n" \
"v_mfma_f32_16x16x32_bf16 v[76:79], v[108:111], v[208:211], v[76:79]\n" \
"ds_read_b64_tr_b16 v[108:109], v231 offset:16384\n" \
"ds_read_b64_tr_b16 v[110:111], v231 offset:20480\n" \
"s_waitcnt vmcnt(12)\n" \
"s_waitcnt lgkmcnt(8)\n" \
"v_mfma_f32_16x16x32_bf16 v[0:3], v[112:115], v[212:215], v[0:3]\n" \
"v_mfma_f32_16x16x32_bf16 v[32:35], v[112:115], v[216:219], v[32:35]\n" \
"v_mfma_f32_16x16x32_bf16 v[64:67], v[112:115], v[220:223], v[64:67]\n" \
"ds_read_b64_tr_b16 v[112:113], v228 offset:24576\n" \
"ds_read_b64_tr_b16 v[114:115], v228 offset:28672\n" \
"v_mfma_f32_16x16x32_bf16 v[4:7], v[116:119], v[212:215], v[4:7]\n" \
"v_mfma_f32_16x16x32_bf16 v[36:39], v[116:119], v[216:219], v[36:39]\n" \
"v_mfma_f32_16x16x32_bf16 v[68:71], v[116:119], v[220:223], v[68:71]\n" \
"ds_read_b64_tr_b16 v[116:117], v229 offset:24576\n" \
"ds_read_b64_tr_b16 v[118:119], v229 offset:28672\n" \
"v_mfma_f32_16x16x32_bf16 v[8:11], v[120:123], v[212:215], v[8:11]\n" \
"v_mfma_f32_16x16x32_bf16 v[40:43], v[120:123], v[216:219], v[40:43]\n" \
"v_mfma_f32_16x16x32_bf16 v[72:75], v[120:123], v[220:223], v[72:75]\n" \
"ds_read_b64_tr_b16 v[120:121], v230 offset:24576\n" \
"ds_read_b64_tr_b16 v[122:123], v230 offset:28672\n" \
"v_mfma_f32_16x16x32_bf16 v[12:15], v[124:127], v[212:215], v[12:15]\n" \
"v_mfma_f32_16x16x32_bf16 v[44:47], v[124:127], v[216:219], v[44:47]\n" \
"v_mfma_f32_16x16x32_bf16 v[76:79], v[124:127], v[220:223], v[76:79]\n" \
"ds_read_b64_tr_b16 v[124:125], v231 offset:24576\n" \
"ds_read_b64_tr_b16 v[126:127], v231 offset:28672\n" \
"s_waitcnt lgkmcnt(8)\n" \
"v_mfma_f32_16x16x32_bf16 v[16:19], v[96:99], v[152:155], v[16:19]\n" \
"v_mfma_f32_16x16x32_bf16 v[48:51], v[96:99], v[156:159], v[48:51]\n" \
"v_mfma_f32_16x16x32_bf16 v[80:83], v[96:99], v[160:163], v[80:83]\n" \
"ds_read_b64_tr_b16 v[96:97], v228 offset:32768\n" \
"ds_read_b64_tr_b16 v[98:99], v228 offset:36864\n" \
"v_mfma_f32_16x16x32_bf16 v[20:23], v[100:103], v[152:155], v[20:23]\n" \
"v_mfma_f32_16x16x32_bf16 v[52:55], v[100:103], v[156:159], v[52:55]\n" \
"v_cvt_pk_bf16_f32 v128, v0, v1\n" \
"v_cvt_pk_bf16_f32 v129, v2, v3\n" \
"v_mfma_f32_16x16x32_bf16 v[84:87], v[100:103], v[160:163], v[84:87]\n" \
"ds_read_b64_tr_b16 v[100:101], v229 offset:32768\n" \
"ds_read_b64_tr_b16 v[102:103], v229 offset:36864\n" \
"v_mfma_f32_16x16x32_bf16 v[24:27], v[104:107], v[152:155], v[24:27]\n" \
"v_cvt_pk_bf16_f32 v130, v4, v5\n" \
"v_cvt_pk_bf16_f32 v131, v6, v7\n" \
"v_mfma_f32_16x16x32_bf16 v[56:59], v[104:107], v[156:159], v[56:59]\n" \
"v_min_u32 v128, -2, v128\n" \
"v_min_u32 v129, -2, v129\n" \
"v_mfma_f32_16x16x32_bf16 v[88:91], v[104:107], v[160:163], v[88:91]\n" \
"ds_read_b64_tr_b16 v[104:105], v230 offset:32768\n" \
"ds_read_b64_tr_b16 v[106:107], v230 offset:36864\n" \
"v_mfma_f32_16x16x32_bf16 v[28:31], v[108:111], v[152:155], v[28:31]\n" \
"v_min_u32 v130, -2, v130\n" \
"v_min_u32 v131, -2, v131\n" \
"v_mfma_f32_16x16x32_bf16 v[60:63], v[108:111], v[156:159], v[60:63]\n" \
"buffer_store_dwordx4 v[128:131], v232, s[64:67], 0 offen offset:0 sc1\n" \
"v_cvt_pk_bf16_f32 v132, v8, v9\n" \
"v_mfma_f32_16x16x32_bf16 v[92:95], v[108:111], v[160:163], v[92:95]\n" \
"ds_read_b64_tr_b16 v[108:109], v231 offset:32768\n" \
"ds_read_b64_tr_b16 v[110:111], v231 offset:36864\n" \
"s_waitcnt lgkmcnt(8)\n" \
"v_mfma_f32_16x16x32_bf16 v[16:19], v[112:115], v[164:167], v[16:19]\n" \
"v_cvt_pk_bf16_f32 v133, v10, v11\n" \
"v_cvt_pk_bf16_f32 v134, v12, v13\n" \
"v_mfma_f32_16x16x32_bf16 v[48:51], v[112:115], v[168:171], v[48:51]\n" \
"v_cvt_pk_bf16_f32 v135, v14, v15\n" \
"v_min_u32 v132, -2, v132\n" \
"v_mfma_f32_16x16x32_bf16 v[80:83], v[112:115], v[172:175], v[80:83]\n" \
"ds_read_b64_tr_b16 v[112:113], v228 offset:40960\n" \
"ds_read_b64_tr_b16 v[114:115], v228 offset:45056\n" \
"v_mfma_f32_16x16x32_bf16 v[20:23], v[116:119], v[164:167], v[20:23]\n" \
"v_min_u32 v133, -2, v133\n" \
"v_min_u32 v134, -2, v134\n" \
"v_mfma_f32_16x16x32_bf16 v[52:55], v[116:119], v[168:171], v[52:55]\n" \
"v_min_u32 v135, -2, v135\n" \
"buffer_store_dwordx4 v[132:135], v232, s[64:67], 0 offen offset:1024 sc1\n" \
"v_mfma_f32_16x16x32_bf16 v[84:87], v[116:119], v[172:175], v[84:87]\n" \
"ds_read_b64_tr_b16 v[116:117], v229 offset:40960\n" \
"ds_read_b64_tr_b16 v[118:119], v229 offset:45056\n" \
"v_mfma_f32_16x16x32_bf16 v[24:27], v[120:123], v[164:167], v[24:27]\n" \
"v_cvt_pk_bf16_f32 v136, v32, v33\n" \
"v_cvt_pk_bf16_f32 v137, v34, v35\n" \
"v_mfma_f32_16x16x32_bf16 v[56:59], v[120:123], v[168:171], v[56:59]\n" \
"v_cvt_pk_bf16_f32 v138, v36, v37\n" \
"v_cvt_pk_bf16_f32 v139, v38, v39\n" \
"v_mfma_f32_16x16x32_bf16 v[88:91], v[120:123], v[172:175], v[88:91]\n" \
"ds_read_b64_tr_b16 v[120:121], v230 offset:40960\n" \
"ds_read_b64_tr_b16 v[122:123], v230 offset:45056\n" \
"v_mfma_f32_16x16x32_bf16 v[28:31], v[124:127], v[164:167], v[28:31]\n" \
"v_min_u32 v136, -2, v136\n" \
"v_min_u32 v137, -2, v137\n" \
"v_mfma_f32_16x16x32_bf16 v[60:63], v[124:127], v[168:171], v[60:63]\n" \
"v_min_u32 v138, -2, v138\n" \
"v_min_u32 v139, -2, v139\n" \
"v_mfma_f32_16x16x32_bf16 v[92:95], v[124:127], v[172:175], v[92:95]\n" \
"ds_read_b64_tr_b16 v[124:125], v231 offset:40960\n" \
"ds_read_b64_tr_b16 v[126:127], v231 offset:45056\n" \
"s_waitcnt lgkmcnt(8)\n" \
"v_mfma_f32_16x16x32_bf16 v[16:19], v[96:99], v[176:179], v[16:19]\n" \
"buffer_store_dwordx4 v[136:139], v232, s[64:67], 0 offen offset:2048 sc1\n" \
"v_cvt_pk_bf16_f32 v140, v40, v41\n" \
"v_mfma_f32_16x16x32_bf16 v[48:51], v[96:99], v[180:183], v[48:51]\n" \
"v_cvt_pk_bf16_f32 v141, v42, v43\n" \
"v_cvt_pk_bf16_f32 v142, v44, v45\n" \
"v_mfma_f32_16x16x32_bf16 v[80:83], v[96:99], v[184:187], v[80:83]\n" \
"ds_read_b64_tr_b16 v[96:97], v228 offset:49152\n" \
"ds_read_b64_tr_b16 v[98:99], v228 offset:53248\n" \
"v_mfma_f32_16x16x32_bf16 v[20:23], v[100:103], v[176:179], v[20:23]\n" \
"v_cvt_pk_bf16_f32 v143, v46, v47\n" \
"v_min_u32 v140, -2, v140\n" \
"v_mfma_f32_16x16x32_bf16 v[52:55], v[100:103], v[180:183], v[52:55]\n" \
"v_min_u32 v141, -2, v141\n" \
"v_min_u32 v142, -2, v142\n" \
"v_mfma_f32_16x16x32_bf16 v[84:87], v[100:103], v[184:187], v[84:87]\n" \
"ds_read_b64_tr_b16 v[100:101], v229 offset:49152\n" \
"ds_read_b64_tr_b16 v[102:103], v229 offset:53248\n" \
"v_mfma_f32_16x16x32_bf16 v[24:27], v[104:107], v[176:179], v[24:27]\n" \
"v_min_u32 v143, -2, v143\n" \
"buffer_store_dwordx4 v[140:143], v232, s[64:67], 0 offen offset:3072 sc1\n" \
"v_mfma_f32_16x16x32_bf16 v[56:59], v[104:107], v[180:183], v[56:59]\n" \
"v_cvt_pk_bf16_f32 v144, v64, v65\n" \
"v_cvt_pk_bf16_f32 v145, v66, v67\n" \
"v_mfma_f32_16x16x32_bf16 v[88:91], v[104:107], v[184:187], v[88:91]\n" \
"ds_read_b64_tr_b16 v[104:105], v230 offset:49152\n" \
"ds_read_b64_tr_b16 v[106:107], v230 offset:53248\n" \
"v_mfma_f32_16x16x32_bf16 v[28:31], v[108:111], v[176:179], v[28:31]\n" \
"v_cvt_pk_bf16_f32 v146, v68, v69\n" \
"v_cvt_pk_bf16_f32 v147, v70, v71\n" \
"v_mfma_f32_16x16x32_bf16 v[60:63], v[108:111], v[180:183], v[60:63]\n" \
"v_min_u32 v144, -2, v144\n" \
"v_min_u32 v145, -2, v145\n" \
"v_mfma_f32_16x16x32_bf16 v[92:95], v[108:111], v[184:187], v[92:95]\n" \
"ds_read_b64_tr_b16 v[108:109], v231 offset:49152\n" \
"ds_read_b64_tr_b16 v[110:111], v231 offset:53248\n" \
"s_waitcnt lgkmcnt(8)\n" \
"v_mfma_f32_16x16x32_bf16 v[16:19], v[112:115], v[188:191], v[16:19]\n" \
"v_min_u32 v146, -2, v146\n" \
"v_min_u32 v147, -2, v147\n" \
"v_mfma_f32_16x16x32_bf16 v[48:51], v[112:115], v[192:195], v[48:51]\n" \
"buffer_store_dwordx4 v[144:147], v232, s[64:67], s68 offen offset:0 sc1\n" \
"v_cvt_pk_bf16_f32 v148, v72, v73\n" \
"v_mfma_f32_16x16x32_bf16 v[80:83], v[112:115], v[196:199], v[80:83]\n" \
"ds_read_b64_tr_b16 v[112:113], v228 offset:57344\n" \
"ds_read_b64_tr_b16 v[114:115], v228 offset:61440\n" \
"v_mfma_f32_16x16x32_bf16 v[20:23], v[116:119], v[188:191], v[20:23]\n" \
"v_cvt_pk_bf16_f32 v149, v74, v75\n" \
"v_cvt_pk_bf16_f32 v150, v76, v77\n" \
"v_mfma_f32_16x16x32_bf16 v[52:55], v[116:119], v[192:195], v[52:55]\n" \
"v_cvt_pk_bf16_f32 v151, v78, v79\n" \
"v_min_u32 v148, -2, v148\n" \
"v_mfma_f32_16x16x32_bf16 v[84:87], v[116:119], v[196:199], v[84:87]\n" \
"ds_read_b64_tr_b16 v[116:117], v229 offset:57344\n" \
"ds_read_b64_tr_b16 v[118:119], v229 offset:61440\n" \
"v_mfma_f32_16x16x32_bf16 v[24:27], v[120:123], v[188:191], v[24:27]\n" \
"v_min_u32 v149, -2, v149\n" \
"v_min_u32 v150, -2, v150\n" \
"v_mfma_f32_16x16x32_bf16 v[56:59], v[120:123], v[192:195], v[56:59]\n" \
"v_min_u32 v151, -2, v151\n" \
"buffer_store_dwordx4 v[148:151], v232, s[64:67], s68 offen offset:1024 sc1\n" \
"v_mfma_f32_16x16x32_bf16 v[88:91], v[120:123], v[196:199], v[88:91]\n" \
"ds_read_b64_tr_b16 v[120:121], v230 offset:57344\n" \
"ds_read_b64_tr_b16 v[122:123], v230 offset:61440\n" \
"v_mfma_f32_16x16x32_bf16 v[28:31], v[124:127], v[188:191], v[28:31]\n" \
"v_mfma_f32_16x16x32_bf16 v[60:63], v[124:127], v[192:195], v[60:63]\n" \
"v_mfma_f32_16x16x32_bf16 v[92:95], v[124:127], v[196:199], v[92:95]\n" \
"ds_read_b64_tr_b16 v[124:125], v231 offset:57344\n" \
"ds_read_b64_tr_b16 v[126:127], v231 offset:61440\n" \
"s_waitcnt lgkmcnt(8)\n" \
"v_mfma_f32_16x16x32_bf16 v[16:19], v[96:99], v[200:203], v[16:19]\n" \
"v_mfma_f32_16x16x32_bf16 v[48:51], v[96:99], v[204:207], v[48:51]\n" \
"v_mfma_f32_16x16x32_bf16 v[80:83], v[96:99], v[208:211], v[80:83]\n" \
"v_mfma_f32_16x16x32_bf16 v[20:23], v[100:103], v[200:203], v[20:23]\n" \
"v_mfma_f32_16x16x32_bf16 v[52:55], v[100:103], v[204:207], v[52:55]\n" \
"v_mfma_f32_16x16x32_bf16 v[84:87], v[100:103], v[208:211], v[84:87]\n" \
"v_mfma_f32_16x16x32_bf16 v[24:27], v[104:107], v[200:203], v[24:27]\n" \
"v_mfma_f32_16x16x32_bf16 v[56:59], v[104:107], v[204:207], v[56:59]\n" \
"v_mfma_f32_16x16x32_bf16 v[88:91], v[104:107], v[208:211], v[88:91]\n" \
"v_mfma_f32_16x16x32_bf16 v[28:31], v[108:111], v[200:203], v[28:31]\n" \
"v_mfma_f32_16x16x32_bf16 v[60:63], v[108:111], v[204:207], v[60:63]\n" \
"v_mfma_f32_16x16x32_bf16 v[92:95], v[108:111], v[208:211], v[92:95]\n" \
"s_waitcnt lgkmcnt(0)\n" \
"v_mfma_f32_16x16x32_bf16 v[16:19], v[112:115], v[212:215], v[16:19]\n" \
"v_mfma_f32_16x16x32_bf16 v[48:51], v[112:115], v[216:219], v[48:51]\n" \
"v_mfma_f32_16x16x32_bf16 v[80:83], v[112:115], v[220:223], v[80:83]\n" \
"v_mfma_f32_16x16x32_bf16 v[20:23], v[116:119], v[212:215], v[20:23]\n" \
"v_mfma_f32_16x16x32_bf16 v[52:55], v[116:119], v[216:219], v[52:55]\n" \
"v_mfma_f32_16x16x32_bf16 v[84:87], v[116:119], v[220:223], v[84:87]\n" \
"v_mfma_f32_16x16x32_bf16 v[24:27], v[120:123], v[212:215], v[24:27]\n" \
"v_mfma_f32_16x16x32_bf16 v[56:59], v[120:123], v[216:219], v[56:59]\n" \
"v_mfma_f32_16x16x32_bf16 v[88:91], v[120:123], v[220:223], v[88:91]\n" \
"v_mfma_f32_16x16x32_bf16 v[28:31], v[124:127], v[212:215], v[28:31]\n" \
"v_mfma_f32_16x16x32_bf16 v[60:63], v[124:127], v[216:219], v[60:63]\n" \
"v_mfma_f32_16x16x32_bf16 v[92:95], v[124:127], v[220:223], v[92:95]\n" \
"s_nop 7\n"
#define GEMM2_ASM \
"s_mov_b32 s59, 0\n" \
"s_mov_b32 s60, 3\n" \
"ds_read_b64_tr_b16 v[96:97], v224 offset:24576\n" \
"ds_read_b64_tr_b16 v[98:99], v224 offset:28672\n" \
"ds_read_b64_tr_b16 v[100:101], v225 offset:24576\n" \
"ds_read_b64_tr_b16 v[102:103], v225 offset:28672\n" \
"ds_read_b64_tr_b16 v[104:105], v226 offset:24576\n" \
"ds_read_b64_tr_b16 v[106:107], v226 offset:28672\n" \
"ds_read_b64_tr_b16 v[108:109], v227 offset:24576\n" \
"ds_read_b64_tr_b16 v[110:111], v227 offset:28672\n" \
"ds_read_b64_tr_b16 v[112:113], v228 offset:24576\n" \
"ds_read_b64_tr_b16 v[114:115], v228 offset:28672\n" \
"ds_read_b64_tr_b16 v[116:117], v229 offset:24576\n" \
"ds_read_b64_tr_b16 v[118:119], v229 offset:28672\n" \
"ds_read_b64_tr_b16 v[120:121], v230 offset:24576\n" \
"ds_read_b64_tr_b16 v[122:123], v230 offset:28672\n" \
"ds_read_b64_tr_b16 v[124:125], v231 offset:24576\n" \
"ds_read_b64_tr_b16 v[126:127], v231 offset:28672\n" \
"v_mov_b32 v0, 0\n" \
"v_mov_b32 v1, 0\n" \
"v_mov_b32 v2, 0\n" \
"v_mov_b32 v3, 0\n" \
"v_mov_b32 v4, 0\n" \
"v_mov_b32 v5, 0\n" \
"v_mov_b32 v6, 0\n" \
"v_mov_b32 v7, 0\n" \
"v_mov_b32 v8, 0\n" \
"v_mov_b32 v9, 0\n" \
"v_mov_b32 v10, 0\n" \
"v_mov_b32 v11, 0\n" \
"v_mov_b32 v12, 0\n" \
"v_mov_b32 v13, 0\n" \
"v_mov_b32 v14, 0\n" \
"v_mov_b32 v15, 0\n" \
"v_mov_b32 v16, 0\n" \
"v_mov_b32 v17, 0\n" \
"v_mov_b32 v18, 0\n" \
"v_mov_b32 v19, 0\n" \
"v_mov_b32 v20, 0\n" \
"v_mov_b32 v21, 0\n" \
"v_mov_b32 v22, 0\n" \
"v_mov_b32 v23, 0\n" \
"v_mov_b32 v24, 0\n" \
"v_mov_b32 v25, 0\n" \
"v_mov_b32 v26, 0\n" \
"v_mov_b32 v27, 0\n" \
"v_mov_b32 v28, 0\n" \
"v_mov_b32 v29, 0\n" \
"v_mov_b32 v30, 0\n" \
"v_mov_b32 v31, 0\n" \
"v_mov_b32 v32, 0\n" \
"v_mov_b32 v33, 0\n" \
"v_mov_b32 v34, 0\n" \
"v_mov_b32 v35, 0\n" \
"v_mov_b32 v36, 0\n" \
"v_mov_b32 v37, 0\n" \
"v_mov_b32 v38, 0\n" \
"v_mov_b32 v39, 0\n" \
"v_mov_b32 v40, 0\n" \
"v_mov_b32 v41, 0\n" \
"v_mov_b32 v42, 0\n" \
"v_mov_b32 v43, 0\n" \
"v_mov_b32 v44, 0\n" \
"v_mov_b32 v45, 0\n" \
"v_mov_b32 v46, 0\n" \
"v_mov_b32 v47, 0\n" \
"v_mov_b32 v48, 0\n" \
"v_mov_b32 v49, 0\n" \
"v_mov_b32 v50, 0\n" \
"v_mov_b32 v51, 0\n" \
"v_mov_b32 v52, 0\n" \
"v_mov_b32 v53, 0\n" \
"v_mov_b32 v54, 0\n" \
"v_mov_b32 v55, 0\n" \
"v_mov_b32 v56, 0\n" \
"v_mov_b32 v57, 0\n" \
"v_mov_b32 v58, 0\n" \
"v_mov_b32 v59, 0\n" \
"v_mov_b32 v60, 0\n" \
"v_mov_b32 v61, 0\n" \
"v_mov_b32 v62, 0\n" \
"v_mov_b32 v63, 0\n" \
"v_mov_b32 v64, 0\n" \
"v_mov_b32 v65, 0\n" \
"v_mov_b32 v66, 0\n" \
"v_mov_b32 v67, 0\n" \
"v_mov_b32 v68, 0\n" \
"v_mov_b32 v69, 0\n" \
"v_mov_b32 v70, 0\n" \
"v_mov_b32 v71, 0\n" \
"v_mov_b32 v72, 0\n" \
"v_mov_b32 v73, 0\n" \
"v_mov_b32 v74, 0\n" \
"v_mov_b32 v75, 0\n" \
"v_mov_b32 v76, 0\n" \
"v_mov_b32 v77, 0\n" \
"v_mov_b32 v78, 0\n" \
"v_mov_b32 v79, 0\n" \
"v_mov_b32 v80, 0\n" \
"v_mov_b32 v81, 0\n" \
"v_mov_b32 v82, 0\n" \
"v_mov_b32 v83, 0\n" \
"v_mov_b32 v84, 0\n" \
"v_mov_b32 v85, 0\n" \
"v_mov_b32 v86, 0\n" \
"v_mov_b32 v87, 0\n" \
"v_mov_b32 v88, 0\n" \
"v_mov_b32 v89, 0\n" \
"v_mov_b32 v90, 0\n" \
"v_mov_b32 v91, 0\n" \
"v_mov_b32 v92, 0\n" \
"v_mov_b32 v93, 0\n" \
"v_mov_b32 v94, 0\n" \
"v_mov_b32 v95, 0\n" \
"s_barrier\n" \
"buffer_load_dwordx4 v[164:167], v232, s[44:47], s59 offen sc1\n" \
"buffer_load_dwordx4 v[168:171], v232, s[48:51], s59 offen sc1\n" \
"buffer_load_dwordx4 v[172:175], v232, s[52:55], s59 offen sc1\n" \
"buffer_load_dwordx4 v[176:179], v232, s[44:47], s59 offen offset:1024 sc1\n" \
"buffer_load_dwordx4 v[180:183], v232, s[48:51], s59 offen offset:1024 sc1\n" \
"buffer_load_dwordx4 v[184:187], v232, s[52:55], s59 offen offset:1024 sc1\n" \
"buffer_load_dwordx4 v[188:191], v232, s[44:47], s59 offen offset:2048 sc1\n" \
"buffer_load_dwordx4 v[192:195], v232, s[48:51], s59 offen offset:2048 sc1\n" \
"buffer_load_dwordx4 v[196:199], v232, s[52:55], s59 offen offset:2048 sc1\n" \
"s_add_u32 s59, s59, 3072\n" \
"s_waitcnt vmcnt(6)\n" \
"s_branch Lg2_%=_in\n" \
"Lg2_%=_loop:\n" \
"s_waitcnt vmcnt(10) lgkmcnt(8)\n" \
"s_barrier\n" \
"v_mfma_f32_16x16x32_bf16 v[0:3], v[128:131], v[96:99], v[0:3]\n" \
"s_add_u32 m0, s56, 49152\n" \
"v_mfma_f32_16x16x32_bf16 v[32:35], v[132:135], v[96:99], v[32:35]\n" \
"buffer_load_dwordx4 v234, s[40:43], s58 offen lds\n" \
"v_mfma_f32_16x16x32_bf16 v[64:67], v[136:139], v[96:99], v[64:67]\n" \
"ds_read_b64_tr_b16 v[96:97], v224 offset:8192\n" \
"ds_read_b64_tr_b16 v[98:99], v224 offset:12288\n" \
"v_mfma_f32_16x16x32_bf16 v[4:7], v[128:131], v[100:103], v[4:7]\n" \
"v_mfma_f32_16x16x32_bf16 v[36:39], v[132:135], v[100:103], v[36:39]\n" \
"v_mfma_f32_16x16x32_bf16 v[68:71], v[136:139], v[100:103], v[68:71]\n" \
"ds_read_b64_tr_b16 v[100:101], v225 offset:8192\n" \
"ds_read_b64_tr_b16 v[102:103], v225 offset:12288\n" \
"v_mfma_f32_16x16x32_bf16 v[8:11], v[128:131], v[104:107], v[8:11]\n" \
"buffer_load_dwordx4 v235, s[40:43], s58 offen offset:1024 lds\n" \
"v_mfma_f32_16x16x32_bf16 v[40:43], v[132:135], v[104:107], v[40:43]\n" \
"s_add_u32 s58, s58, s61\n" \
"v_mfma_f32_16x16x32_bf16 v[72:75], v[136:139], v[104:107], v[72:75]\n" \
"ds_read_b64_tr_b16 v[104:105], v226 offset:8192\n" \
"ds_read_b64_tr_b16 v[106:107], v226 offset:12288\n" \
"v_mfma_f32_16x16x32_bf16 v[12:15], v[128:131], v[108:111], v[12:15]\n" \
"v_mfma_f32_16x16x32_bf16 v[44:47], v[132:135], v[108:111], v[44:47]\n" \
"buffer_load_dwordx4 v[164:167], v232, s[44:47], s59 offen sc1\n" \
"v_mfma_f32_16x16x32_bf16 v[76:79], v[136:139], v[108:111], v[76:79]\n" \
"ds_read_b64_tr_b16 v[108:109], v227 offset:8192\n" \
"ds_read_b64_tr_b16 v[110:111], v227 offset:12288\n" \
"s_waitcnt lgkmcnt(8)\n" \
"v_mfma_f32_16x16x32_bf16 v[16:19], v[128:131], v[112:115], v[16:19]\n" \
"v_mfma_f32_16x16x32_bf16 v[48:51], v[132:135], v[112:115], v[48:51]\n" \
"v_mfma_f32_16x16x32_bf16 v[80:83], v[136:139], v[112:115], v[80:83]\n" \
"ds_read_b64_tr_b16 v[112:113], v228 offset:8192\n" \
"ds_read_b64_tr_b16 v[114:115], v228 offset:12288\n" \
"v_mfma_f32_16x16x32_bf16 v[20:23], v[128:131], v[116:119], v[20:23]\n" \
"buffer_load_dwordx4 v[168:171], v232, s[48:51], s59 offen sc1\n" \
"v_mfma_f32_16x16x32_bf16 v[52:55], v[132:135], v[116:119], v[52:55]\n" \
"v_mfma_f32_16x16x32_bf16 v[84:87], v[136:139], v[116:119], v[84:87]\n" \
"ds_read_b64_tr_b16 v[116:117], v229 offset:8192\n" \
"ds_read_b64_tr_b16 v[118:119], v229 offset:12288\n" \
"v_mfma_f32_16x16x32_bf16 v[24:27], v[128:131], v[120:123], v[24:27]\n" \
"v_mfma_f32_16x16x32_bf16 v[56:59], v[132:135], v[120:123], v[56:59]\n" \
"buffer_load_dwordx4 v[172:175], v232, s[52:55], s59 offen sc1\n" \
"v_mfma_f32_16x16x32_bf16 v[88:91], v[136:139], v[120:123], v[88:91]\n" \
"ds_read_b64_tr_b16 v[120:121], v230 offset:8192\n" \
"ds_read_b64_tr_b16 v[122:123], v230 offset:12288\n" \
"v_mfma_f32_16x16x32_bf16 v[28:31], v[128:131], v[124:127], v[28:31]\n" \
"s_add_u32 s59, s59, 1024\n" \
"v_mfma_f32_16x16x32_bf16 v[60:63], v[132:135], v[124:127], v[60:63]\n" \
"v_mfma_f32_16x16x32_bf16 v[92:95], v[136:139], v[124:127], v[92:95]\n" \
"ds_read_b64_tr_b16 v[124:125], v231 offset:8192\n" \
"ds_read_b64_tr_b16 v[126:127], v231 offset:12288\n" \
"s_waitcnt vmcnt(10) lgkmcnt(8)\n" \
"v_mfma_f32_16x16x32_bf16 v[0:3], v[140:143], v[96:99], v[0:3]\n" \
"s_add_u32 m0, s56, 57344\n" \
"v_mfma_f32_16x16x32_bf16 v[32:35], v[144:147], v[96:99], v[32:35]\n" \
"buffer_load_dwordx4 v234, s[40:43], s58 offen lds\n" \
"v_mfma_f32_16x16x32_bf16 v[64:67], v[148:151], v[96:99], v[64:67]\n" \
"ds_read_b64_tr_b16 v[96:97], v224 offset:16384\n" \
"ds_read_b64_tr_b16 v[98:99], v224 offset:20480\n" \
"v_mfma_f32_16x16x32_bf16 v[4:7], v[140:143], v[100:103], v[4:7]\n" \
"v_mfma_f32_16x16x32_bf16 v[36:39], v[144:147], v[100:103], v[36:39]\n" \
"v_mfma_f32_16x16x32_bf16 v[68:71], v[148:151], v[100:103], v[68:71]\n" \
"ds_read_b64_tr_b16 v[100:101], v225 offset:16384\n" \
"ds_read_b64_tr_b16 v[102:103], v225 offset:20480\n" \
"v_mfma_f32_16x16x32_bf16 v[8:11], v[140:143], v[104:107], v[8:11]\n" \
"buffer_load_dwordx4 v235, s[40:43], s58 offen offset:1024 lds\n" \
"v_mfma_f32_16x16x32_bf16 v[40:43], v[144:147], v[104:107], v[40:43]\n" \
"s_add_u32 s58, s58, s61\n" \
"v_mfma_f32_16x16x32_bf16 v[72:75], v[148:151], v[104:107], v[72:75]\n" \
"ds_read_b64_tr_b16 v[104:105], v226 offset:16384\n" \
"ds_read_b64_tr_b16 v[106:107], v226 offset:20480\n" \
"v_mfma_f32_16x16x32_bf16 v[12:15], v[140:143], v[108:111], v[12:15]\n" \
"v_mfma_f32_16x16x32_bf16 v[44:47], v[144:147], v[108:111], v[44:47]\n" \
"buffer_load_dwordx4 v[176:179], v232, s[44:47], s59 offen sc1\n" \
"v_mfma_f32_16x16x32_bf16 v[76:79], v[148:151], v[108:111], v[76:79]\n" \
"ds_read_b64_tr_b16 v[108:109], v227 offset:16384\n" \
"ds_read_b64_tr_b16 v[110:111], v227 offset:20480\n" \
"s_waitcnt lgkmcnt(8)\n" \
"v_mfma_f32_16x16x32_bf16 v[16:19], v[140:143], v[112:115], v[16:19]\n" \
"v_mfma_f32_16x16x32_bf16 v[48:51], v[144:147], v[112:115], v[48:51]\n" \
"v_mfma_f32_16x16x32_bf16 v[80:83], v[148:151], v[112:115], v[80:83]\n" \
"ds_read_b64_tr_b16 v[112:113], v228 offset:16384\n" \
"ds_read_b64_tr_b16 v[114:115], v228 offset:20480\n" \
"v_mfma_f32_16x16x32_bf16 v[20:23], v[140:143], v[116:119], v[20:23]\n" \
"buffer_load_dwordx4 v[180:183], v232, s[48:51], s59 offen sc1\n" \
"v_mfma_f32_16x16x32_bf16 v[52:55], v[144:147], v[116:119], v[52:55]\n" \
"v_mfma_f32_16x16x32_bf16 v[84:87], v[148:151], v[116:119], v[84:87]\n" \
"ds_read_b64_tr_b16 v[116:117], v229 offset:16384\n" \
"ds_read_b64_tr_b16 v[118:119], v229 offset:20480\n" \
"v_mfma_f32_16x16x32_bf16 v[24:27], v[140:143], v[120:123], v[24:27]\n" \
"v_mfma_f32_16x16x32_bf16 v[56:59], v[144:147], v[120:123], v[56:59]\n" \
"buffer_load_dwordx4 v[184:187], v232, s[52:55], s59 offen sc1\n" \
"v_mfma_f32_16x16x32_bf16 v[88:91], v[148:151], v[120:123], v[88:91]\n" \
"ds_read_b64_tr_b16 v[120:121], v230 offset:16384\n" \
"ds_read_b64_tr_b16 v[122:123], v230 offset:20480\n" \
"v_mfma_f32_16x16x32_bf16 v[28:31], v[140:143], v[124:127], v[28:31]\n" \
"s_add_u32 s59, s59, 1024\n" \
"v_mfma_f32_16x16x32_bf16 v[60:63], v[144:147], v[124:127], v[60:63]\n" \
"v_mfma_f32_16x16x32_bf16 v[92:95], v[148:151], v[124:127], v[92:95]\n" \
"ds_read_b64_tr_b16 v[124:125], v231 offset:16384\n" \
"ds_read_b64_tr_b16 v[126:127], v231 offset:20480\n" \
"s_waitcnt vmcnt(10) lgkmcnt(8)\n" \
"s_barrier\n" \
"v_mfma_f32_16x16x32_bf16 v[0:3], v[152:155], v[96:99], v[0:3]\n" \
"s_add_u32 m0, s56, 0\n" \
"v_mfma_f32_16x16x32_bf16 v[32:35], v[156:159], v[96:99], v[32:35]\n" \
"buffer_load_dwordx4 v234, s[40:43], s58 offen lds\n" \
"v_mfma_f32_16x16x32_bf16 v[64:67], v[160:163], v[96:99], v[64:67]\n" \
"ds_read_b64_tr_b16 v[96:97], v224 offset:24576\n" \
"ds_read_b64_tr_b16 v[98:99], v224 offset:28672\n" \
"v_mfma_f32_16x16x32_bf16 v[4:7], v[152:155], v[100:103], v[4:7]\n" \
"v_mfma_f32_16x16x32_bf16 v[36:39], v[156:159], v[100:103], v[36:39]\n" \
"v_mfma_f32_16x16x32_bf16 v[68:71], v[160:163], v[100:103], v[68:71]\n" \
"ds_read_b64_tr_b16 v[100:101], v225 offset:24576\n" \
"ds_read_b64_tr_b16 v[102:103], v225 offset:28672\n" \
"v_mfma_f32_16x16x32_bf16 v[8:11], v[152:155], v[104:107], v[8:11]\n" \
"buffer_load_dwordx4 v235, s[40:43], s58 offen offset:1024 lds\n" \
"v_mfma_f32_16x16x32_bf16 v[40:43], v[156:159], v[104:107], v[40:43]\n" \
"s_add_u32 s58, s58, s61\n" \
"v_mfma_f32_16x16x32_bf16 v[72:75], v[160:163], v[104:107], v[72:75]\n" \
"ds_read_b64_tr_b16 v[104:105], v226 offset:24576\n" \
"ds_read_b64_tr_b16 v[106:107], v226 offset:28672\n" \
"v_mfma_f32_16x16x32_bf16 v[12:15], v[152:155], v[108:111], v[12:15]\n" \
"v_mfma_f32_16x16x32_bf16 v[44:47], v[156:159], v[108:111], v[44:47]\n" \
"buffer_load_dwordx4 v[188:191], v232, s[44:47], s59 offen sc1\n" \
"v_mfma_f32_16x16x32_bf16 v[76:79], v[160:163], v[108:111], v[76:79]\n" \
"ds_read_b64_tr_b16 v[108:109], v227 offset:24576\n" \
"ds_read_b64_tr_b16 v[110:111], v227 offset:28672\n" \
"s_waitcnt lgkmcnt(8)\n" \
"v_mfma_f32_16x16x32_bf16 v[16:19], v[152:155], v[112:115], v[16:19]\n" \
"v_mfma_f32_16x16x32_bf16 v[48:51], v[156:159], v[112:115], v[48:51]\n" \
"v_mfma_f32_16x16x32_bf16 v[80:83], v[160:163], v[112:115], v[80:83]\n" \
"ds_read_b64_tr_b16 v[112:113], v228 offset:24576\n" \
"ds_read_b64_tr_b16 v[114:115], v228 offset:28672\n" \
"v_mfma_f32_16x16x32_bf16 v[20:23], v[152:155], v[116:119], v[20:23]\n" \
"buffer_load_dwordx4 v[192:195], v232, s[48:51], s59 offen sc1\n" \
"v_mfma_f32_16x16x32_bf16 v[52:55], v[156:159], v[116:119], v[52:55]\n" \
"v_mfma_f32_16x16x32_bf16 v[84:87], v[160:163], v[116:119], v[84:87]\n" \
"ds_read_b64_tr_b16 v[116:117], v229 offset:24576\n" \
"ds_read_b64_tr_b16 v[118:119], v229 offset:28672\n" \
"v_mfma_f32_16x16x32_bf16 v[24:27], v[152:155], v[120:123], v[24:27]\n" \
"v_mfma_f32_16x16x32_bf16 v[56:59], v[156:159], v[120:123], v[56:59]\n" \
"buffer_load_dwordx4 v[196:199], v232, s[52:55], s59 offen sc1\n" \
"v_mfma_f32_16x16x32_bf16 v[88:91], v[160:163], v[120:123], v[88:91]\n" \
"ds_read_b64_tr_b16 v[120:121], v230 offset:24576\n" \
"ds_read_b64_tr_b16 v[122:123], v230 offset:28672\n" \
"v_mfma_f32_16x16x32_bf16 v[28:31], v[152:155], v[124:127], v[28:31]\n" \
"s_add_u32 s59, s59, 1024\n" \
"v_mfma_f32_16x16x32_bf16 v[60:63], v[156:159], v[124:127], v[60:63]\n" \
"v_mfma_f32_16x16x32_bf16 v[92:95], v[160:163], v[124:127], v[92:95]\n" \
"ds_read_b64_tr_b16 v[124:125], v231 offset:24576\n" \
"ds_read_b64_tr_b16 v[126:127], v231 offset:28672\n" \
"Lg2_%=_in:\n" \
"s_waitcnt vmcnt(10) lgkmcnt(8)\n" \
"v_mfma_f32_16x16x32_bf16 v[0:3], v[164:167], v[96:99], v[0:3]\n" \
"s_add_u32 m0, s56, 8192\n" \
"v_mfma_f32_16x16x32_bf16 v[32:35], v[168:171], v[96:99], v[32:35]\n" \
"buffer_load_dwordx4 v234, s[40:43], s58 offen lds\n" \
"v_mfma_f32_16x16x32_bf16 v[64:67], v[172:175], v[96:99], v[64:67]\n" \
"ds_read_b64_tr_b16 v[96:97], v224 offset:32768\n" \
"ds_read_b64_tr_b16 v[98:99], v224 offset:36864\n" \
"v_mfma_f32_16x16x32_bf16 v[4:7], v[164:167], v[100:103], v[4:7]\n" \
"v_mfma_f32_16x16x32_bf16 v[36:39], v[168:171], v[100:103], v[36:39]\n" \
"v_mfma_f32_16x16x32_bf16 v[68:71], v[172:175], v[100:103], v[68:71]\n" \
"ds_read_b64_tr_b16 v[100:101], v225 offset:32768\n" \
"ds_read_b64_tr_b16 v[102:103], v225 offset:36864\n" \
"v_mfma_f32_16x16x32_bf16 v[8:11], v[164:167], v[104:107], v[8:11]\n" \
"buffer_load_dwordx4 v235, s[40:43], s58 offen offset:1024 lds\n" \
"v_mfma_f32_16x16x32_bf16 v[40:43], v[168:171], v[104:107], v[40:43]\n" \
"s_add_u32 s58, s58, s61\n" \
"v_mfma_f32_16x16x32_bf16 v[72:75], v[172:175], v[104:107], v[72:75]\n" \
"ds_read_b64_tr_b16 v[104:105], v226 offset:32768\n" \
"ds_read_b64_tr_b16 v[106:107], v226 offset:36864\n" \
"v_mfma_f32_16x16x32_bf16 v[12:15], v[164:167], v[108:111], v[12:15]\n" \
"v_mfma_f32_16x16x32_bf16 v[44:47], v[168:171], v[108:111], v[44:47]\n" \
"buffer_load_dwordx4 v[200:203], v232, s[44:47], s59 offen sc1\n" \
"v_mfma_f32_16x16x32_bf16 v[76:79], v[172:175], v[108:111], v[76:79]\n" \
"ds_read_b64_tr_b16 v[108:109], v227 offset:32768\n" \
"ds_read_b64_tr_b16 v[110:111], v227 offset:36864\n" \
"s_waitcnt lgkmcnt(8)\n" \
"v_mfma_f32_16x16x32_bf16 v[16:19], v[164:167], v[112:115], v[16:19]\n" \
"v_mfma_f32_16x16x32_bf16 v[48:51], v[168:171], v[112:115], v[48:51]\n" \
"v_mfma_f32_16x16x32_bf16 v[80:83], v[172:175], v[112:115], v[80:83]\n" \
"ds_read_b64_tr_b16 v[112:113], v228 offset:32768\n" \
"ds_read_b64_tr_b16 v[114:115], v228 offset:36864\n" \
"v_mfma_f32_16x16x32_bf16 v[20:23], v[164:167], v[116:119], v[20:23]\n" \
"buffer_load_dwordx4 v[204:207], v232, s[48:51], s59 offen sc1\n" \
"v_mfma_f32_16x16x32_bf16 v[52:55], v[168:171], v[116:119], v[52:55]\n" \
"v_mfma_f32_16x16x32_bf16 v[84:87], v[172:175], v[116:119], v[84:87]\n" \
"ds_read_b64_tr_b16 v[116:117], v229 offset:32768\n" \
"ds_read_b64_tr_b16 v[118:119], v229 offset:36864\n" \
"v_mfma_f32_16x16x32_bf16 v[24:27], v[164:167], v[120:123], v[24:27]\n" \
"v_mfma_f32_16x16x32_bf16 v[56:59], v[168:171], v[120:123], v[56:59]\n" \
"buffer_load_dwordx4 v[208:211], v232, s[52:55], s59 offen sc1\n" \
"v_mfma_f32_16x16x32_bf16 v[88:91], v[172:175], v[120:123], v[88:91]\n" \
"ds_read_b64_tr_b16 v[120:121], v230 offset:32768\n" \
"ds_read_b64_tr_b16 v[122:123], v230 offset:36864\n" \
"v_mfma_f32_16x16x32_bf16 v[28:31], v[164:167], v[124:127], v[28:31]\n" \
"s_add_u32 s59, s59, 1024\n" \
"v_mfma_f32_16x16x32_bf16 v[60:63], v[168:171], v[124:127], v[60:63]\n" \
"v_mfma_f32_16x16x32_bf16 v[92:95], v[172:175], v[124:127], v[92:95]\n" \
"ds_read_b64_tr_b16 v[124:125], v231 offset:32768\n" \
"ds_read_b64_tr_b16 v[126:127], v231 offset:36864\n" \
"s_waitcnt vmcnt(8) lgkmcnt(8)\n" \
"s_barrier\n" \
"v_mfma_f32_16x16x32_bf16 v[0:3], v[176:179], v[96:99], v[0:3]\n" \
"s_add_u32 m0, s56, 16384\n" \
"v_mfma_f32_16x16x32_bf16 v[32:35], v[180:183], v[96:99], v[32:35]\n" \
"buffer_load_dwordx4 v234, s[40:43], s58 offen lds\n" \
"v_mfma_f32_16x16x32_bf16 v[64:67], v[184:187], v[96:99], v[64:67]\n" \
"ds_read_b64_tr_b16 v[96:97], v224 offset:40960\n" \
"ds_read_b64_tr_b16 v[98:99], v224 offset:45056\n" \
"v_mfma_f32_16x16x32_bf16 v[4:7], v[176:179], v[100:103], v[4:7]\n" \
"v_mfma_f32_16x16x32_bf16 v[36:39], v[180:183], v[100:103], v[36:39]\n" \
"v_mfma_f32_16x16x32_bf16 v[68:71], v[184:187], v[100:103], v[68:71]\n" \
"ds_read_b64_tr_b16 v[100:101], v225 offset:40960\n" \
"ds_read_b64_tr_b16 v[102:103], v225 offset:45056\n" \
"v_mfma_f32_16x16x32_bf16 v[8:11], v[176:179], v[104:107], v[8:11]\n" \
"buffer_load_dwordx4 v235, s[40:43], s58 offen offset:1024 lds\n" \
"v_mfma_f32_16x16x32_bf16 v[40:43], v[180:183], v[104:107], v[40:43]\n" \
"s_add_u32 s58, s58, s61\n" \
"v_mfma_f32_16x16x32_bf16 v[72:75], v[184:187], v[104:107], v[72:75]\n" \
"ds_read_b64_tr_b16 v[104:105], v226 offset:40960\n" \
"ds_read_b64_tr_b16 v[106:107], v226 offset:45056\n" \
"v_mfma_f32_16x16x32_bf16 v[12:15], v[176:179], v[108:111], v[12:15]\n" \
"v_mfma_f32_16x16x32_bf16 v[44:47], v[180:183], v[108:111], v[44:47]\n" \
"buffer_load_dwordx4 v[212:215], v232, s[44:47], s59 offen sc1\n" \
"v_mfma_f32_16x16x32_bf16 v[76:79], v[184:187], v[108:111], v[76:79]\n" \
"ds_read_b64_tr_b16 v[108:109], v227 offset:40960\n" \
"ds_read_b64_tr_b16 v[110:111], v227 offset:45056\n" \
"s_waitcnt lgkmcnt(8)\n" \
"v_mfma_f32_16x16x32_bf16 v[16:19], v[176:179], v[112:115], v[16:19]\n" \
"v_mfma_f32_16x16x32_bf16 v[48:51], v[180:183], v[112:115], v[48:51]\n" \
"v_mfma_f32_16x16x32_bf16 v[80:83], v[184:187], v[112:115], v[80:83]\n" \
"ds_read_b64_tr_b16 v[112:113], v228 offset:40960\n" \
"ds_read_b64_tr_b16 v[114:115], v228 offset:45056\n" \
"v_mfma_f32_16x16x32_bf16 v[20:23], v[176:179], v[116:119], v[20:23]\n" \
"buffer_load_dwordx4 v[216:219], v232, s[48:51], s59 offen sc1\n" \
"v_mfma_f32_16x16x32_bf16 v[52:55], v[180:183], v[116:119], v[52:55]\n" \
"v_mfma_f32_16x16x32_bf16 v[84:87], v[184:187], v[116:119], v[84:87]\n" \
"ds_read_b64_tr_b16 v[116:117], v229 offset:40960\n" \
"ds_read_b64_tr_b16 v[118:119], v229 offset:45056\n" \
"v_mfma_f32_16x16x32_bf16 v[24:27], v[176:179], v[120:123], v[24:27]\n" \
"v_mfma_f32_16x16x32_bf16 v[56:59], v[180:183], v[120:123], v[56:59]\n" \
"buffer_load_dwordx4 v[220:223], v232, s[52:55], s59 offen sc1\n" \
"v_mfma_f32_16x16x32_bf16 v[88:91], v[184:187], v[120:123], v[88:91]\n" \
"ds_read_b64_tr_b16 v[120:121], v230 offset:40960\n" \
"ds_read_b64_tr_b16 v[122:123], v230 offset:45056\n" \
"v_mfma_f32_16x16x32_bf16 v[28:31], v[176:179], v[124:127], v[28:31]\n" \
"s_add_u32 s59, s59, 1024\n" \
"v_mfma_f32_16x16x32_bf16 v[60:63], v[180:183], v[124:127], v[60:63]\n" \
"v_mfma_f32_16x16x32_bf16 v[92:95], v[184:187], v[124:127], v[92:95]\n" \
"ds_read_b64_tr_b16 v[124:125], v231 offset:40960\n" \
"ds_read_b64_tr_b16 v[126:127], v231 offset:45056\n" \
"s_waitcnt vmcnt(10) lgkmcnt(8)\n" \
"v_mfma_f32_16x16x32_bf16 v[0:3], v[188:191], v[96:99], v[0:3]\n" \
"s_add_u32 m0, s56, 24576\n" \
"v_mfma_f32_16x16x32_bf16 v[32:35], v[192:195], v[96:99], v[32:35]\n" \
"buffer_load_dwordx4 v234, s[40:43], s58 offen lds\n" \
"v_mfma_f32_16x16x32_bf16 v[64:67], v[196:199], v[96:99], v[64:67]\n" \
"ds_read_b64_tr_b16 v[96:97], v224 offset:49152\n" \
"ds_read_b64_tr_b16 v[98:99], v224 offset:53248\n" \
"v_mfma_f32_16x16x32_bf16 v[4:7], v[188:191], v[100:103], v[4:7]\n" \
"v_mfma_f32_16x16x32_bf16 v[36:39], v[192:195], v[100:103], v[36:39]\n" \
"v_mfma_f32_16x16x32_bf16 v[68:71], v[196:199], v[100:103], v[68:71]\n" \
"ds_read_b64_tr_b16 v[100:101], v225 offset:49152\n" \
"ds_read_b64_tr_b16 v[102:103], v225 offset:53248\n" \
"v_mfma_f32_16x16x32_bf16 v[8:11], v[188:191], v[104:107], v[8:11]\n" \
"buffer_load_dwordx4 v235, s[40:43], s58 offen offset:1024 lds\n" \
"v_mfma_f32_16x16x32_bf16 v[40:43], v[192:195], v[104:107], v[40:43]\n" \
"s_add_u32 s58, s58, s61\n" \
"v_mfma_f32_16x16x32_bf16 v[72:75], v[196:199], v[104:107], v[72:75]\n" \
"ds_read_b64_tr_b16 v[104:105], v226 offset:49152\n" \
"ds_read_b64_tr_b16 v[106:107], v226 offset:53248\n" \
"v_mfma_f32_16x16x32_bf16 v[12:15], v[188:191], v[108:111], v[12:15]\n" \
"v_mfma_f32_16x16x32_bf16 v[44:47], v[192:195], v[108:111], v[44:47]\n" \
"buffer_load_dwordx4 v[128:131], v232, s[44:47], s59 offen sc1\n" \
"v_mfma_f32_16x16x32_bf16 v[76:79], v[196:199], v[108:111], v[76:79]\n" \
"ds_read_b64_tr_b16 v[108:109], v227 offset:49152\n" \
"ds_read_b64_tr_b16 v[110:111], v227 offset:53248\n" \
"s_waitcnt lgkmcnt(8)\n" \
"v_mfma_f32_16x16x32_bf16 v[16:19], v[188:191], v[112:115], v[16:19]\n" \
"v_mfma_f32_16x16x32_bf16 v[48:51], v[192:195], v[112:115], v[48:51]\n" \
"v_mfma_f32_16x16x32_bf16 v[80:83], v[196:199], v[112:115], v[80:83]\n" \
"ds_read_b64_tr_b16 v[112:113], v228 offset:49152\n" \
"ds_read_b64_tr_b16 v[114:115], v228 offset:53248\n" \
"v_mfma_f32_16x16x32_bf16 v[20:23], v[188:191], v[116:119], v[20:23]\n" \
"buffer_load_dwordx4 v[132:135], v232, s[48:51], s59 offen sc1\n" \
"v_mfma_f32_16x16x32_bf16 v[52:55], v[192:195], v[116:119], v[52:55]\n" \
"v_mfma_f32_16x16x32_bf16 v[84:87], v[196:199], v[116:119], v[84:87]\n" \
"ds_read_b64_tr_b16 v[116:117], v229 offset:49152\n" \
"ds_read_b64_tr_b16 v[118:119], v229 offset:53248\n" \
"v_mfma_f32_16x16x32_bf16 v[24:27], v[188:191], v[120:123], v[24:27]\n" \
"v_mfma_f32_16x16x32_bf16 v[56:59], v[192:195], v[120:123], v[56:59]\n" \
"buffer_load_dwordx4 v[136:139], v232, s[52:55], s59 offen sc1\n" \
"v_mfma_f32_16x16x32_bf16 v[88:91], v[196:199], v[120:123], v[88:91]\n" \
"ds_read_b64_tr_b16 v[120:121], v230 offset:49152\n" \
"ds_read_b64_tr_b16 v[122:123], v230 offset:53248\n" \
"v_mfma_f32_16x16x32_bf16 v[28:31], v[188:191], v[124:127], v[28:31]\n" \
"s_add_u32 s59, s59, 1024\n" \
"v_mfma_f32_16x16x32_bf16 v[60:63], v[192:195], v[124:127], v[60:63]\n" \
"v_mfma_f32_16x16x32_bf16 v[92:95], v[196:199], v[124:127], v[92:95]\n" \
"ds_read_b64_tr_b16 v[124:125], v231 offset:49152\n" \
"ds_read_b64_tr_b16 v[126:127], v231 offset:53248\n" \
"s_waitcnt vmcnt(10) lgkmcnt(8)\n" \
"s_barrier\n" \
"v_mfma_f32_16x16x32_bf16 v[0:3], v[200:203], v[96:99], v[0:3]\n" \
"s_add_u32 m0, s56, 32768\n" \
"v_mfma_f32_16x16x32_bf16 v[32:35], v[204:207], v[96:99], v[32:35]\n" \
"buffer_load_dwordx4 v234, s[40:43], s58 offen lds\n" \
"v_mfma_f32_16x16x32_bf16 v[64:67], v[208:211], v[96:99], v[64:67]\n" \
"ds_read_b64_tr_b16 v[96:97], v224 offset:57344\n" \
"ds_read_b64_tr_b16 v[98:99], v224 offset:61440\n" \
"v_mfma_f32_16x16x32_bf16 v[4:7], v[200:203], v[100:103], v[4:7]\n" \
"v_mfma_f32_16x16x32_bf16 v[36:39], v[204:207], v[100:103], v[36:39]\n" \
"v_mfma_f32_16x16x32_bf16 v[68:71], v[208:211], v[100:103], v[68:71]\n" \
"ds_read_b64_tr_b16 v[100:101], v225 offset:57344\n" \
"ds_read_b64_tr_b16 v[102:103], v225 offset:61440\n" \
"v_mfma_f32_16x16x32_bf16 v[8:11], v[200:203], v[104:107], v[8:11]\n" \
"buffer_load_dwordx4 v235, s[40:43], s58 offen offset:1024 lds\n" \
"v_mfma_f32_16x16x32_bf16 v[40:43], v[204:207], v[104:107], v[40:43]\n" \
"s_add_u32 s58, s58, s61\n" \
"v_mfma_f32_16x16x32_bf16 v[72:75], v[208:211], v[104:107], v[72:75]\n" \
"ds_read_b64_tr_b16 v[104:105], v226 offset:57344\n" \
"ds_read_b64_tr_b16 v[106:107], v226 offset:61440\n" \
"v_mfma_f32_16x16x32_bf16 v[12:15], v[200:203], v[108:111], v[12:15]\n" \
"v_mfma_f32_16x16x32_bf16 v[44:47], v[204:207], v[108:111], v[44:47]\n" \
"buffer_load_dwordx4 v[140:143], v232, s[44:47], s59 offen sc1\n" \
"v_mfma_f32_16x16x32_bf16 v[76:79], v[208:211], v[108:111], v[76:79]\n" \
"ds_read_b64_tr_b16 v[108:109], v227 offset:57344\n" \
"ds_read_b64_tr_b16 v[110:111], v227 offset:61440\n" \
"s_waitcnt lgkmcnt(8)\n" \
"v_mfma_f32_16x16x32_bf16 v[16:19], v[200:203], v[112:115], v[16:19]\n" \
"v_mfma_f32_16x16x32_bf16 v[48:51], v[204:207], v[112:115], v[48:51]\n" \
"v_mfma_f32_16x16x32_bf16 v[80:83], v[208:211], v[112:115], v[80:83]\n" \
"ds_read_b64_tr_b16 v[112:113], v228 offset:57344\n" \
"ds_read_b64_tr_b16 v[114:115], v228 offset:61440\n" \
"v_mfma_f32_16x16x32_bf16 v[20:23], v[200:203], v[116:119], v[20:23]\n" \
"buffer_load_dwordx4 v[144:147], v232, s[48:51], s59 offen sc1\n" \
"v_mfma_f32_16x16x32_bf16 v[52:55], v[204:207], v[116:119], v[52:55]\n" \
"v_mfma_f32_16x16x32_bf16 v[84:87], v[208:211], v[116:119], v[84:87]\n" \
"ds_read_b64_tr_b16 v[116:117], v229 offset:57344\n" \
"ds_read_b64_tr_b16 v[118:119], v229 offset:61440\n" \
"v_mfma_f32_16x16x32_bf16 v[24:27], v[200:203], v[120:123], v[24:27]\n" \
"v_mfma_f32_16x16x32_bf16 v[56:59], v[204:207], v[120:123], v[56:59]\n" \
"buffer_load_dwordx4 v[148:151], v232, s[52:55], s59 offen sc1\n" \
"v_mfma_f32_16x16x32_bf16 v[88:91], v[208:211], v[120:123], v[88:91]\n" \
"ds_read_b64_tr_b16 v[120:121], v230 offset:57344\n" \
"ds_read_b64_tr_b16 v[122:123], v230 offset:61440\n" \
"v_mfma_f32_16x16x32_bf16 v[28:31], v[200:203], v[124:127], v[28:31]\n" \
"s_add_u32 s59, s59, 1024\n" \
"v_mfma_f32_16x16x32_bf16 v[60:63], v[204:207], v[124:127], v[60:63]\n" \
"v_mfma_f32_16x16x32_bf16 v[92:95], v[208:211], v[124:127], v[92:95]\n" \
"ds_read_b64_tr_b16 v[124:125], v231 offset:57344\n" \
"ds_read_b64_tr_b16 v[126:127], v231 offset:61440\n" \
"s_waitcnt vmcnt(10) lgkmcnt(8)\n" \
"v_mfma_f32_16x16x32_bf16 v[0:3], v[212:215], v[96:99], v[0:3]\n" \
"s_add_u32 m0, s56, 40960\n" \
"v_mfma_f32_16x16x32_bf16 v[32:35], v[216:219], v[96:99], v[32:35]\n" \
"buffer_load_dwordx4 v234, s[40:43], s58 offen lds\n" \
"v_mfma_f32_16x16x32_bf16 v[64:67], v[220:223], v[96:99], v[64:67]\n" \
"ds_read_b64_tr_b16 v[96:97], v224 offset:0\n" \
"ds_read_b64_tr_b16 v[98:99], v224 offset:4096\n" \
"v_mfma_f32_16x16x32_bf16 v[4:7], v[212:215], v[100:103], v[4:7]\n" \
"v_mfma_f32_16x16x32_bf16 v[36:39], v[216:219], v[100:103], v[36:39]\n" \
"v_mfma_f32_16x16x32_bf16 v[68:71], v[220:223], v[100:103], v[68:71]\n" \
"ds_read_b64_tr_b16 v[100:101], v225 offset:0\n" \
"ds_read_b64_tr_b16 v[102:103], v225 offset:4096\n" \
"v_mfma_f32_16x16x32_bf16 v[8:11], v[212:215], v[104:107], v[8:11]\n" \
"buffer_load_dwordx4 v235, s[40:43], s58 offen offset:1024 lds\n" \
"v_mfma_f32_16x16x32_bf16 v[40:43], v[216:219], v[104:107], v[40:43]\n" \
"s_add_u32 s58, s58, s61\n" \
"v_mfma_f32_16x16x32_bf16 v[72:75], v[220:223], v[104:107], v[72:75]\n" \
"ds_read_b64_tr_b16 v[104:105], v226 offset:0\n" \
"ds_read_b64_tr_b16 v[106:107], v226 offset:4096\n" \
"v_mfma_f32_16x16x32_bf16 v[12:15], v[212:215], v[108:111], v[12:15]\n" \
"v_mfma_f32_16x16x32_bf16 v[44:47], v[216:219], v[108:111], v[44:47]\n" \
"buffer_load_dwordx4 v[152:155], v232, s[44:47], s59 offen sc1\n" \
"v_mfma_f32_16x16x32_bf16 v[76:79], v[220:223], v[108:111], v[76:79]\n" \
"ds_read_b64_tr_b16 v[108:109], v227 offset:0\n" \
"ds_read_b64_tr_b16 v[110:111], v227 offset:4096\n" \
"s_waitcnt lgkmcnt(8)\n" \
"v_mfma_f32_16x16x32_bf16 v[16:19], v[212:215], v[112:115], v[16:19]\n" \
"v_mfma_f32_16x16x32_bf16 v[48:51], v[216:219], v[112:115], v[48:51]\n" \
"v_mfma_f32_16x16x32_bf16 v[80:83], v[220:223], v[112:115], v[80:83]\n" \
"ds_read_b64_tr_b16 v[112:113], v228 offset:0\n" \
"ds_read_b64_tr_b16 v[114:115], v228 offset:4096\n" \
"v_mfma_f32_16x16x32_bf16 v[20:23], v[212:215], v[116:119], v[20:23]\n" \
"buffer_load_dwordx4 v[156:159], v232, s[48:51], s59 offen sc1\n" \
"v_mfma_f32_16x16x32_bf16 v[52:55], v[216:219], v[116:119], v[52:55]\n" \
"v_mfma_f32_16x16x32_bf16 v[84:87], v[220:223], v[116:119], v[84:87]\n" \
"ds_read_b64_tr_b16 v[116:117], v229 offset:0\n" \
"ds_read_b64_tr_b16 v[118:119], v229 offset:4096\n" \
"v_mfma_f32_16x16x32_bf16 v[24:27], v[212:215], v[120:123], v[24:27]\n" \
"v_mfma_f32_16x16x32_bf16 v[56:59], v[216:219], v[120:123], v[56:59]\n" \
"buffer_load_dwordx4 v[160:163], v232, s[52:55], s59 offen sc1\n" \
"v_mfma_f32_16x16x32_bf16 v[88:91], v[220:223], v[120:123], v[88:91]\n" \
"ds_read_b64_tr_b16 v[120:121], v230 offset:0\n" \
"ds_read_b64_tr_b16 v[122:123], v230 offset:4096\n" \
"v_mfma_f32_16x16x32_bf16 v[28:31], v[212:215], v[124:127], v[28:31]\n" \
"s_add_u32 s59, s59, 1024\n" \
"s_sub_u32 s60, s60, 1\n" \
"v_mfma_f32_16x16x32_bf16 v[60:63], v[216:219], v[124:127], v[60:63]\n" \
"s_cmp_eq_u32 s60, 0\n" \
"v_mfma_f32_16x16x32_bf16 v[92:95], v[220:223], v[124:127], v[92:95]\n" \
"ds_read_b64_tr_b16 v[124:125], v231 offset:0\n" \
"ds_read_b64_tr_b16 v[126:127], v231 offset:4096\n" \
"s_cbranch_scc0 Lg2_%=_loop\n" \
"s_waitcnt vmcnt(8) lgkmcnt(8)\n" \
"s_barrier\n" \
"v_mfma_f32_16x16x32_bf16 v[0:3], v[128:131], v[96:99], v[0:3]\n" \
"s_add_u32 m0, s56, 49152\n" \
"v_mfma_f32_16x16x32_bf16 v[32:35], v[132:135], v[96:99], v[32:35]\n" \
"buffer_load_dwordx4 v234, s[40:43], s58 offen lds\n" \
"v_mfma_f32_16x16x32_bf16 v[64:67], v[136:139], v[96:99], v[64:67]\n" \
"ds_read_b64_tr_b16 v[96:97], v224 offset:8192\n" \
"ds_read_b64_tr_b16 v[98:99], v224 offset:12288\n" \
"v_mfma_f32_16x16x32_bf16 v[4:7], v[128:131], v[100:103], v[4:7]\n" \
"v_mfma_f32_16x16x32_bf16 v[36:39], v[132:135], v[100:103], v[36:39]\n" \
"v_mfma_f32_16x16x32_bf16 v[68:71], v[136:139], v[100:103], v[68:71]\n" \
"ds_read_b64_tr_b16 v[100:101], v225 offset:8192\n" \
"ds_read_b64_tr_b16 v[102:103], v225 offset:12288\n" \
"v_mfma_f32_16x16x32_bf16 v[8:11], v[128:131], v[104:107], v[8:11]\n" \
"buffer_load_dwordx4 v235, s[40:43], s58 offen offset:1024 lds\n" \
"v_mfma_f32_16x16x32_bf16 v[40:43], v[132:135], v[104:107], v[40:43]\n" \
"s_add_u32 s58, s58, s61\n" \
"v_mfma_f32_16x16x32_bf16 v[72:75], v[136:139], v[104:107], v[72:75]\n" \
"ds_read_b64_tr_b16 v[104:105], v226 offset:8192\n" \
"ds_read_b64_tr_b16 v[106:107], v226 offset:12288\n" \
"v_mfma_f32_16x16x32_bf16 v[12:15], v[128:131], v[108:111], v[12:15]\n" \
"buffer_load_dwordx4 v[164:167], v232, s[44:47], s59 offen sc1\n" \
"v_mfma_f32_16x16x32_bf16 v[44:47], v[132:135], v[108:111], v[44:47]\n" \
"v_mfma_f32_16x16x32_bf16 v[76:79], v[136:139], v[108:111], v[76:79]\n" \
"ds_read_b64_tr_b16 v[108:109], v227 offset:8192\n" \
"ds_read_b64_tr_b16 v[110:111], v227 offset:12288\n" \
"s_waitcnt lgkmcnt(8)\n" \
"v_mfma_f32_16x16x32_bf16 v[16:19], v[128:131], v[112:115], v[16:19]\n" \
"buffer_load_dwordx4 v[168:171], v232, s[48:51], s59 offen sc1\n" \
"v_mfma_f32_16x16x32_bf16 v[48:51], v[132:135], v[112:115], v[48:51]\n" \
"v_mfma_f32_16x16x32_bf16 v[80:83], v[136:139], v[112:115], v[80:83]\n" \
"ds_read_b64_tr_b16 v[112:113], v228 offset:8192\n" \
"ds_read_b64_tr_b16 v[114:115], v228 offset:12288\n" \
"v_mfma_f32_16x16x32_bf16 v[20:23], v[128:131], v[116:119], v[20:23]\n" \
"buffer_load_dwordx4 v[172:175], v232, s[52:55], s59 offen sc1\n" \
"v_mfma_f32_16x16x32_bf16 v[52:55], v[132:135], v[116:119], v[52:55]\n" \
"v_mfma_f32_16x16x32_bf16 v[84:87], v[136:139], v[116:119], v[84:87]\n" \
"ds_read_b64_tr_b16 v[116:117], v229 offset:8192\n" \
"ds_read_b64_tr_b16 v[118:119], v229 offset:12288\n" \
"v_mfma_f32_16x16x32_bf16 v[24:27], v[128:131], v[120:123], v[24:27]\n" \
"buffer_load_dwordx4 v[176:179], v232, s[44:47], s59 offen offset:1024 sc1\n" \
"v_mfma_f32_16x16x32_bf16 v[56:59], v[132:135], v[120:123], v[56:59]\n" \
"v_mfma_f32_16x16x32_bf16 v[88:91], v[136:139], v[120:123], v[88:91]\n" \
"ds_read_b64_tr_b16 v[120:121], v230 offset:8192\n" \
"ds_read_b64_tr_b16 v[122:123], v230 offset:12288\n" \
"v_mfma_f32_16x16x32_bf16 v[28:31], v[128:131], v[124:127], v[28:31]\n" \
"buffer_load_dwordx4 v[180:183], v232, s[48:51], s59 offen offset:1024 sc1\n" \
"v_mfma_f32_16x16x32_bf16 v[60:63], v[132:135], v[124:127], v[60:63]\n" \
"buffer_load_dwordx4 v[184:187], v232, s[52:55], s59 offen offset:1024 sc1\n" \
"v_mfma_f32_16x16x32_bf16 v[92:95], v[136:139], v[124:127], v[92:95]\n" \
"ds_read_b64_tr_b16 v[124:125], v231 offset:8192\n" \
"ds_read_b64_tr_b16 v[126:127], v231 offset:12288\n" \
"s_waitcnt vmcnt(13) lgkmcnt(8)\n" \
"v_mfma_f32_16x16x32_bf16 v[0:3], v[140:143], v[96:99], v[0:3]\n" \
"s_add_u32 m0, s56, 57344\n" \
"v_mfma_f32_16x16x32_bf16 v[32:35], v[144:147], v[96:99], v[32:35]\n" \
"buffer_load_dwordx4 v234, s[40:43], s58 offen lds\n" \
"v_mfma_f32_16x16x32_bf16 v[64:67], v[148:151], v[96:99], v[64:67]\n" \
"ds_read_b64_tr_b16 v[96:97], v224 offset:16384\n" \
"ds_read_b64_tr_b16 v[98:99], v224 offset:20480\n" \
"v_mfma_f32_16x16x32_bf16 v[4:7], v[140:143], v[100:103], v[4:7]\n" \
"v_mfma_f32_16x16x32_bf16 v[36:39], v[144:147], v[100:103], v[36:39]\n" \
"v_mfma_f32_16x16x32_bf16 v[68:71], v[148:151], v[100:103], v[68:71]\n" \
"ds_read_b64_tr_b16 v[100:101], v225 offset:16384\n" \
"ds_read_b64_tr_b16 v[102:103], v225 offset:20480\n" \
"v_mfma_f32_16x16x32_bf16 v[8:11], v[140:143], v[104:107], v[8:11]\n" \
"buffer_load_dwordx4 v235, s[40:43], s58 offen offset:1024 lds\n" \
"v_mfma_f32_16x16x32_bf16 v[40:43], v[144:147], v[104:107], v[40:43]\n" \
"s_add_u32 s58, s58, s61\n" \
"v_mfma_f32_16x16x32_bf16 v[72:75], v[148:151], v[104:107], v[72:75]\n" \
"ds_read_b64_tr_b16 v[104:105], v226 offset:16384\n" \
"ds_read_b64_tr_b16 v[106:107], v226 offset:20480\n" \
"v_mfma_f32_16x16x32_bf16 v[12:15], v[140:143], v[108:111], v[12:15]\n" \
"buffer_load_dwordx4 v[188:191], v232, s[44:47], s59 offen offset:2048 sc1\n" \
"v_mfma_f32_16x16x32_bf16 v[44:47], v[144:147], v[108:111], v[44:47]\n" \
"v_mfma_f32_16x16x32_bf16 v[76:79], v[148:151], v[108:111], v[76:79]\n" \
"ds_read_b64_tr_b16 v[108:109], v224 offset:24576\n" \
"ds_read_b64_tr_b16 v[110:111], v224 offset:28672\n" \
"s_waitcnt lgkmcnt(8)\n" \
"v_mfma_f32_16x16x32_bf16 v[16:19], v[140:143], v[112:115], v[16:19]\n" \
"buffer_load_dwordx4 v[192:195], v232, s[48:51], s59 offen offset:2048 sc1\n" \
"v_mfma_f32_16x16x32_bf16 v[48:51], v[144:147], v[112:115], v[48:51]\n" \
"v_mfma_f32_16x16x32_bf16 v[80:83], v[148:151], v[112:115], v[80:83]\n" \
"ds_read_b64_tr_b16 v[112:113], v225 offset:24576\n" \
"ds_read_b64_tr_b16 v[114:115], v225 offset:28672\n" \
"v_mfma_f32_16x16x32_bf16 v[20:23], v[140:143], v[116:119], v[20:23]\n" \
"buffer_load_dwordx4 v[196:199], v232, s[52:55], s59 offen offset:2048 sc1\n" \
"v_mfma_f32_16x16x32_bf16 v[52:55], v[144:147], v[116:119], v[52:55]\n" \
"v_mfma_f32_16x16x32_bf16 v[84:87], v[148:151], v[116:119], v[84:87]\n" \
"ds_read_b64_tr_b16 v[116:117], v226 offset:24576\n" \
"ds_read_b64_tr_b16 v[118:119], v226 offset:28672\n" \
"v_mfma_f32_16x16x32_bf16 v[24:27], v[140:143], v[120:123], v[24:27]\n" \
"buffer_load_dwordx4 v[200:203], v232, s[44:47], s59 offen offset:3072 sc1\n" \
"v_mfma_f32_16x16x32_bf16 v[56:59], v[144:147], v[120:123], v[56:59]\n" \
"v_mfma_f32_16x16x32_bf16 v[88:91], v[148:151], v[120:123], v[88:91]\n" \
"ds_read_b64_tr_b16 v[120:121], v224 offset:32768\n" \
"ds_read_b64_tr_b16 v[122:123], v224 offset:36864\n" \
"v_mfma_f32_16x16x32_bf16 v[28:31], v[140:143], v[124:127], v[28:31]\n" \
"buffer_load_dwordx4 v[204:207], v232, s[48:51], s59 offen offset:3072 sc1\n" \
"v_mfma_f32_16x16x32_bf16 v[60:63], v[144:147], v[124:127], v[60:63]\n" \
"buffer_load_dwordx4 v[208:211], v232, s[52:55], s59 offen offset:3072 sc1\n" \
"v_mfma_f32_16x16x32_bf16 v[92:95], v[148:151], v[124:127], v[92:95]\n" \
"ds_read_b64_tr_b16 v[124:125], v225 offset:32768\n" \
"ds_read_b64_tr_b16 v[126:127], v225 offset:36864\n" \
"s_waitcnt vmcnt(14)\n" \
"s_barrier\n" \
"s_add_u32 s59, s59, 4096\n" \
"s_waitcnt lgkmcnt(8)\n" \
"v_mfma_f32_16x16x32_bf16 v[0:3], v[152:155], v[96:99], v[0:3]\n" \
"buffer_load_dwordx4 v[212:215], v232, s[44:47], s59 offen sc1\n" \
"v_mfma_f32_16x16x32_bf16 v[32:35], v[156:159], v[96:99], v[32:35]\n" \
"v_mfma_f32_16x16x32_bf16 v[64:67], v[160:163], v[96:99], v[64:67]\n" \
"ds_read_b64_tr_b16 v[96:97], v226 offset:32768\n" \
"ds_read_b64_tr_b16 v[98:99], v226 offset:36864\n" \
"v_mfma_f32_16x16x32_bf16 v[4:7], v[152:155], v[100:103], v[4:7]\n" \
"buffer_load_dwordx4 v[216:219], v232, s[48:51], s59 offen sc1\n" \
"v_mfma_f32_16x16x32_bf16 v[36:39], v[156:159], v[100:103], v[36:39]\n" \
"v_mfma_f32_16x16x32_bf16 v[68:71], v[160:163], v[100:103], v[68:71]\n" \
"ds_read_b64_tr_b16 v[100:101], v224 offset:40960\n" \
"ds_read_b64_tr_b16 v[102:103], v224 offset:45056\n" \
"v_mfma_f32_16x16x32_bf16 v[8:11], v[152:155], v[104:107], v[8:11]\n" \
"buffer_load_dwordx4 v[220:223], v232, s[52:55], s59 offen sc1\n" \
"v_mfma_f32_16x16x32_bf16 v[40:43], v[156:159], v[104:107], v[40:43]\n" \
"v_mfma_f32_16x16x32_bf16 v[72:75], v[160:163], v[104:107], v[72:75]\n" \
"ds_read_b64_tr_b16 v[104:105], v225 offset:40960\n" \
"ds_read_b64_tr_b16 v[106:107], v225 offset:45056\n" \
"s_waitcnt vmcnt(14)\n" \
"v_mfma_f32_16x16x32_bf16 v[0:3], v[164:167], v[108:111], v[0:3]\n" \
"v_mfma_f32_16x16x32_bf16 v[32:35], v[168:171], v[108:111], v[32:35]\n" \
"v_mfma_f32_16x16x32_bf16 v[64:67], v[172:175], v[108:111], v[64:67]\n" \
"ds_read_b64_tr_b16 v[108:109], v226 offset:40960\n" \
"ds_read_b64_tr_b16 v[110:111], v226 offset:45056\n" \
"s_waitcnt lgkmcnt(8)\n" \
"v_mfma_f32_16x16x32_bf16 v[4:7], v[164:167], v[112:115], v[4:7]\n" \
"v_mfma_f32_16x16x32_bf16 v[36:39], v[168:171], v[112:115], v[36:39]\n" \
"v_mfma_f32_16x16x32_bf16 v[68:71], v[172:175], v[112:115], v[68:71]\n" \
"ds_read_b64_tr_b16 v[112:113], v224 offset:49152\n" \
"ds_read_b64_tr_b16 v[114:115], v224 offset:53248\n" \
"v_mfma_f32_16x16x32_bf16 v[8:11], v[164:167], v[116:119], v[8:11]\n" \
"v_mfma_f32_16x16x32_bf16 v[40:43], v[168:171], v[116:119], v[40:43]\n" \
"v_mfma_f32_16x16x32_bf16 v[72:75], v[172:175], v[116:119], v[72:75]\n" \
"ds_read_b64_tr_b16 v[116:117], v225 offset:49152\n" \
"ds_read_b64_tr_b16 v[118:119], v225 offset:53248\n" \
"s_waitcnt vmcnt(9)\n" \
"s_barrier\n" \
"v_mfma_f32_16x16x32_bf16 v[0:3], v[176:179], v[120:123], v[0:3]\n" \
"v_mfma_f32_16x16x32_bf16 v[32:35], v[180:183], v[120:123], v[32:35]\n" \
"v_mfma_f32_16x16x32_bf16 v[64:67], v[184:187], v[120:123], v[64:67]\n" \
"ds_read_b64_tr_b16 v[120:121], v226 offset:49152\n" \
"ds_read_b64_tr_b16 v[122:123], v226 offset:53248\n" \
"v_mfma_f32_16x16x32_bf16 v[4:7], v[176:179], v[124:127], v[4:7]\n" \
"v_mfma_f32_16x16x32_bf16 v[36:39], v[180:183], v[124:127], v[36:39]\n" \
"v_mfma_f32_16x16x32_bf16 v[68:71], v[184:187], v[124:127], v[68:71]\n" \
"ds_read_b64_tr_b16 v[124:125], v224 offset:57344\n" \
"ds_read_b64_tr_b16 v[126:127], v224 offset:61440\n" \
"s_waitcnt lgkmcnt(8)\n" \
"v_mfma_f32_16x16x32_bf16 v[8:11], v[176:179], v[96:99], v[8:11]\n" \
"v_mfma_f32_16x16x32_bf16 v[40:43], v[180:183], v[96:99], v[40:43]\n" \
"v_mfma_f32_16x16x32_bf16 v[72:75], v[184:187], v[96:99], v[72:75]\n" \
"ds_read_b64_tr_b16 v[96:97], v225 offset:57344\n" \
"ds_read_b64_tr_b16 v[98:99], v225 offset:61440\n" \
"s_waitcnt vmcnt(6)\n" \
"v_mfma_f32_16x16x32_bf16 v[0:3], v[188:191], v[100:103], v[0:3]\n" \
"v_mfma_f32_16x16x32_bf16 v[32:35], v[192:195], v[100:103], v[32:35]\n" \
"v_mfma_f32_16x16x32_bf16 v[64:67], v[196:199], v[100:103], v[64:67]\n" \
"ds_read_b64_tr_b16 v[100:101], v226 offset:57344\n" \
"ds_read_b64_tr_b16 v[102:103], v226 offset:61440\n" \
"v_mfma_f32_16x16x32_bf16 v[4:7], v[188:191], v[104:107], v[4:7]\n" \
"v_mfma_f32_16x16x32_bf16 v[36:39], v[192:195], v[104:107], v[36:39]\n" \
"v_mfma_f32_16x16x32_bf16 v[68:71], v[196:199], v[104:107], v[68:71]\n" \
"ds_read_b64_tr_b16 v[104:105], v227 offset:16384\n" \
"ds_read_b64_tr_b16 v[106:107], v227 offset:20480\n" \
"v_mfma_f32_16x16x32_bf16 v[8:11], v[188:191], v[108:111], v[8:11]\n" \
"v_mfma_f32_16x16x32_bf16 v[40:43], v[192:195], v[108:111], v[40:43]\n" \
"v_mfma_f32_16x16x32_bf16 v[72:75], v[196:199], v[108:111], v[72:75]\n" \
"ds_read_b64_tr_b16 v[108:109], v228 offset:16384\n" \
"ds_read_b64_tr_b16 v[110:111], v228 offset:20480\n" \
"s_waitcnt vmcnt(3)\n" \
"s_waitcnt lgkmcnt(8)\n" \
"v_mfma_f32_16x16x32_bf16 v[0:3], v[200:203], v[112:115], v[0:3]\n" \
"v_mfma_f32_16x16x32_bf16 v[32:35], v[204:207], v[112:115], v[32:35]\n" \
"v_mfma_f32_16x16x32_bf16 v[64:67], v[208:211], v[112:115], v[64:67]\n" \
"ds_read_b64_tr_b16 v[112:113], v229 offset:16384\n" \
"ds_read_b64_tr_b16 v[114:115], v229 offset:20480\n" \
"v_mfma_f32_16x16x32_bf16 v[4:7], v[200:203], v[116:119], v[4:7]\n" \
"v_mfma_f32_16x16x32_bf16 v[36:39], v[204:207], v[116:119], v[36:39]\n" \
"v_mfma_f32_16x16x32_bf16 v[68:71], v[208:211], v[116:119], v[68:71]\n" \
"ds_read_b64_tr_b16 v[116:117], v227 offset:24576\n" \
"ds_read_b64_tr_b16 v[118:119], v227 offset:28672\n" \
"v_mfma_f32_16x16x32_bf16 v[8:11], v[200:203], v[120:123], v[8:11]\n" \
"v_mfma_f32_16x16x32_bf16 v[40:43], v[204:207], v[120:123], v[40:43]\n" \
"v_mfma_f32_16x16x32_bf16 v[72:75], v[208:211], v[120:123], v[72:75]\n" \
"ds_read_b64_tr_b16 v[120:121], v228 offset:24576\n" \
"ds_read_b64_tr_b16 v[122:123], v228 offset:28672\n" \
"s_waitcnt vmcnt(0)\n" \
"v_mfma_f32_16x16x32_bf16 v[0:3], v[212:215], v[124:127], v[0:3]\n" \
"v_mfma_f32_16x16x32_bf16 v[32:35], v[216:219], v[124:127], v[32:35]\n" \
"v_mfma_f32_16x16x32_bf16 v[64:67], v[220:223], v[124:127], v[64:67]\n" \
"ds_read_b64_tr_b16 v[124:125], v229 offset:24576\n" \
"ds_read_b64_tr_b16 v[126:127], v229 offset:28672\n" \
"s_waitcnt lgkmcnt(8)\n" \
"v_mfma_f32_16x16x32_bf16 v[4:7], v[212:215], v[96:99], v[4:7]\n" \
"v_mfma_f32_16x16x32_bf16 v[36:39], v[216:219], v[96:99], v[36:39]\n" \
"v_mfma_f32_16x16x32_bf16 v[68:71], v[220:223], v[96:99], v[68:71]\n" \
"ds_read_b64_tr_b16 v[96:97], v227 offset:32768\n" \
"ds_read_b64_tr_b16 v[98:99], v227 offset:36864\n" \
"v_mfma_f32_16x16x32_bf16 v[8:11], v[212:215], v[100:103], v[8:11]\n" \
"v_mfma_f32_16x16x32_bf16 v[40:43], v[216:219], v[100:103], v[40:43]\n" \
"v_mfma_f32_16x16x32_bf16 v[72:75], v[220:223], v[100:103], v[72:75]\n" \
"ds_read_b64_tr_b16 v[100:101], v228 offset:32768\n" \
"ds_read_b64_tr_b16 v[102:103], v228 offset:36864\n" \
"v_mfma_f32_16x16x32_bf16 v[12:15], v[152:155], v[104:107], v[12:15]\n" \
"v_mfma_f32_16x16x32_bf16 v[44:47], v[156:159], v[104:107], v[44:47]\n" \
"v_mfma_f32_16x16x32_bf16 v[76:79], v[160:163], v[104:107], v[76:79]\n" \
"ds_read_b64_tr_b16 v[104:105], v229 offset:32768\n" \
"ds_read_b64_tr_b16 v[106:107], v229 offset:36864\n" \
"v_mfma_f32_16x16x32_bf16 v[16:19], v[152:155], v[108:111], v[16:19]\n" \
"v_mfma_f32_16x16x32_bf16 v[48:51], v[156:159], v[108:111], v[48:51]\n" \
"v_cvt_pk_bf16_f32 v128, v0, v1\n" \
"v_cvt_pk_bf16_f32 v129, v2, v3\n" \
"v_mfma_f32_16x16x32_bf16 v[80:83], v[160:163], v[108:111], v[80:83]\n" \
"ds_read_b64_tr_b16 v[108:109], v227 offset:40960\n" \
"ds_read_b64_tr_b16 v[110:111], v227 offset:45056\n" \
"s_waitcnt lgkmcnt(8)\n" \
"v_mfma_f32_16x16x32_bf16 v[20:23], v[152:155], v[112:115], v[20:23]\n" \
"v_cvt_pk_bf16_f32 v130, v32, v33\n" \
"v_cvt_pk_bf16_f32 v131, v34, v35\n" \
"v_mfma_f32_16x16x32_bf16 v[52:55], v[156:159], v[112:115], v[52:55]\n" \
"v_cvt_pk_bf16_f32 v132, v64, v65\n" \
"v_cvt_pk_bf16_f32 v133, v66, v67\n" \
"v_mfma_f32_16x16x32_bf16 v[84:87], v[160:163], v[112:115], v[84:87]\n" \
"ds_read_b64_tr_b16 v[112:113], v228 offset:40960\n" \
"ds_read_b64_tr_b16 v[114:115], v228 offset:45056\n" \
"v_mfma_f32_16x16x32_bf16 v[12:15], v[164:167], v[116:119], v[12:15]\n" \
"v_min_u32 v128, -2, v128\n" \
"v_min_u32 v129, -2, v129\n" \
"v_mfma_f32_16x16x32_bf16 v[44:47], v[168:171], v[116:119], v[44:47]\n" \
"v_min_u32 v130, -2, v130\n" \
"v_min_u32 v131, -2, v131\n" \
"v_mfma_f32_16x16x32_bf16 v[76:79], v[172:175], v[116:119], v[76:79]\n" \
"ds_read_b64_tr_b16 v[116:117], v229 offset:40960\n" \
"ds_read_b64_tr_b16 v[118:119], v229 offset:45056\n" \
"v_mfma_f32_16x16x32_bf16 v[16:19], v[164:167], v[120:123], v[16:19]\n" \
"v_min_u32 v132, -2, v132\n" \
"v_min_u32 v133, -2, v133\n" \
"v_mfma_f32_16x16x32_bf16 v[48:51], v[168:171], v[120:123], v[48:51]\n" \
"buffer_store_dwordx4 v[128:131], v232, s[64:67], s68 offen sc1\n" \
"buffer_store_dwordx2 v[132:133], v233, s[64:67], s68 offen sc1\n" \
"v_mfma_f32_16x16x32_bf16 v[80:83], v[172:175], v[120:123], v[80:83]\n" \
"ds_read_b64_tr_b16 v[120:121], v227 offset:49152\n" \
"ds_read_b64_tr_b16 v[122:123], v227 offset:53248\n" \
"v_mfma_f32_16x16x32_bf16 v[20:23], v[164:167], v[124:127], v[20:23]\n" \
"v_cvt_pk_bf16_f32 v134, v4, v5\n" \
"v_cvt_pk_bf16_f32 v135, v6, v7\n" \
"v_mfma_f32_16x16x32_bf16 v[52:55], v[168:171], v[124:127], v[52:55]\n" \
"v_cvt_pk_bf16_f32 v136, v36, v37\n" \
"v_cvt_pk_bf16_f32 v137, v38, v39\n" \
"v_mfma_f32_16x16x32_bf16 v[84:87], v[172:175], v[124:127], v[84:87]\n" \
"ds_read_b64_tr_b16 v[124:125], v228 offset:49152\n" \
"ds_read_b64_tr_b16 v[126:127], v228 offset:53248\n" \
"s_waitcnt lgkmcnt(8)\n" \
"v_mfma_f32_16x16x32_bf16 v[12:15], v[176:179], v[96:99], v[12:15]\n" \
"v_cvt_pk_bf16_f32 v138, v68, v69\n" \
"v_cvt_pk_bf16_f32 v139, v70, v71\n" \
"v_mfma_f32_16x16x32_bf16 v[44:47], v[180:183], v[96:99], v[44:47]\n" \
"v_min_u32 v134, -2, v134\n" \
"v_min_u32 v135, -2, v135\n" \
"v_mfma_f32_16x16x32_bf16 v[76:79], v[184:187], v[96:99], v[76:79]\n" \
"ds_read_b64_tr_b16 v[96:97], v229 offset:49152\n" \
"ds_read_b64_tr_b16 v[98:99], v229 offset:53248\n" \
"v_mfma_f32_16x16x32_bf16 v[16:19], v[176:179], v[100:103], v[16:19]\n" \
"v_min_u32 v136, -2, v136\n" \
"v_min_u32 v137, -2, v137\n" \
"v_mfma_f32_16x16x32_bf16 v[48:51], v[180:183], v[100:103], v[48:51]\n" \
"v_min_u32 v138, -2, v138\n" \
"v_min_u32 v139, -2, v139\n" \
"v_mfma_f32_16x16x32_bf16 v[80:83], v[184:187], v[100:103], v[80:83]\n" \
"ds_read_b64_tr_b16 v[100:101], v227 offset:57344\n" \
"ds_read_b64_tr_b16 v[102:103], v227 offset:61440\n" \
"v_mfma_f32_16x16x32_bf16 v[20:23], v[176:179], v[104:107], v[20:23]\n" \
"buffer_store_dwordx4 v[134:137], v232, s[64:67], s69 offen sc1\n" \
"buffer_store_dwordx2 v[138:139], v233, s[64:67], s69 offen sc1\n" \
"v_mfma_f32_16x16x32_bf16 v[52:55], v[180:183], v[104:107], v[52:55]\n" \
"v_cvt_pk_bf16_f32 v140, v8, v9\n" \
"v_cvt_pk_bf16_f32 v141, v10, v11\n" \
"v_mfma_f32_16x16x32_bf16 v[84:87], v[184:187], v[104:107], v[84:87]\n" \
"ds_read_b64_tr_b16 v[104:105], v228 offset:57344\n" \
"ds_read_b64_tr_b16 v[106:107], v228 offset:61440\n" \
"v_mfma_f32_16x16x32_bf16 v[12:15], v[188:191], v[108:111], v[12:15]\n" \
"v_cvt_pk_bf16_f32 v142, v40, v41\n" \
"v_cvt_pk_bf16_f32 v143, v42, v43\n" \
"v_mfma_f32_16x16x32_bf16 v[44:47], v[192:195], v[108:111], v[44:47]\n" \
"v_cvt_pk_bf16_f32 v144, v72, v73\n" \
"v_cvt_pk_bf16_f32 v145, v74, v75\n" \
"v_mfma_f32_16x16x32_bf16 v[76:79], v[196:199], v[108:111], v[76:79]\n" \
"ds_read_b64_tr_b16 v[108:109], v229 offset:57344\n" \
"ds_read_b64_tr_b16 v[110:111], v229 offset:61440\n" \
"s_waitcnt lgkmcnt(8)\n" \
"v_mfma_f32_16x16x32_bf16 v[16:19], v[188:191], v[112:115], v[16:19]\n" \
"v_min_u32 v140, -2, v140\n" \
"v_min_u32 v141, -2, v141\n" \
"v_mfma_f32_16x16x32_bf16 v[48:51], v[192:195], v[112:115], v[48:51]\n" \
"v_min_u32 v142, -2, v142\n" \
"v_min_u32 v143, -2, v143\n" \
"v_mfma_f32_16x16x32_bf16 v[80:83], v[196:199], v[112:115], v[80:83]\n" \
"ds_read_b64_tr_b16 v[112:113], v230 offset:16384\n" \
"ds_read_b64_tr_b16 v[114:115], v230 offset:20480\n" \
"v_mfma_f32_16x16x32_bf16 v[20:23], v[188:191], v[116:119], v[20:23]\n" \
"v_min_u32 v144, -2, v144\n" \
"v_min_u32 v145, -2, v145\n" \
"v_mfma_f32_16x16x32_bf16 v[52:55], v[192:195], v[116:119], v[52:55]\n" \
"buffer_store_dwordx4 v[140:143], v232, s[64:67], s70 offen sc1\n" \
"buffer_store_dwordx2 v[144:145], v233, s[64:67], s70 offen sc1\n" \
"v_mfma_f32_16x16x32_bf16 v[84:87], v[196:199], v[116:119], v[84:87]\n" \
"ds_read_b64_tr_b16 v[116:117], v231 offset:16384\n" \
"ds_read_b64_tr_b16 v[118:119], v231 offset:20480\n" \
"v_mfma_f32_16x16x32_bf16 v[12:15], v[200:203], v[120:123], v[12:15]\n" \
"v_mfma_f32_16x16x32_bf16 v[44:47], v[204:207], v[120:123], v[44:47]\n" \
"v_mfma_f32_16x16x32_bf16 v[76:79], v[208:211], v[120:123], v[76:79]\n" \
"ds_read_b64_tr_b16 v[120:121], v230 offset:24576\n" \
"ds_read_b64_tr_b16 v[122:123], v230 offset:28672\n" \
"v_mfma_f32_16x16x32_bf16 v[16:19], v[200:203], v[124:127], v[16:19]\n" \
"v_mfma_f32_16x16x32_bf16 v[48:51], v[204:207], v[124:127], v[48:51]\n" \
"v_mfma_f32_16x16x32_bf16 v[80:83], v[208:211], v[124:127], v[80:83]\n" \
"ds_read_b64_tr_b16 v[124:125], v231 offset:24576\n" \
"ds_read_b64_tr_b16 v[126:127], v231 offset:28672\n" \
"s_waitcnt lgkmcnt(8)\n" \
"v_mfma_f32_16x16x32_bf16 v[20:23], v[200:203], v[96:99], v[20:23]\n" \
"v_mfma_f32_16x16x32_bf16 v[52:55], v[204:207], v[96:99], v[52:55]\n" \
"v_mfma_f32_16x16x32_bf16 v[84:87], v[208:211], v[96:99], v[84:87]\n" \
"ds_read_b64_tr_b16 v[96:97], v230 offset:32768\n" \
"ds_read_b64_tr_b16 v[98:99], v230 offset:36864\n" \
"v_mfma_f32_16x16x32_bf16 v[12:15], v[212:215], v[100:103], v[12:15]\n" \
"v_mfma_f32_16x16x32_bf16 v[44:47], v[216:219], v[100:103], v[44:47]\n" \
"v_mfma_f32_16x16x32_bf16 v[76:79], v[220:223], v[100:103], v[76:79]\n" \
"ds_read_b64_tr_b16 v[100:101], v231 offset:32768\n" \
"ds_read_b64_tr_b16 v[102:103], v231 offset:36864\n" \
"v_mfma_f32_16x16x32_bf16 v[16:19], v[212:215], v[104:107], v[16:19]\n" \
"v_mfma_f32_16x16x32_bf16 v[48:51], v[216:219], v[104:107], v[48:51]\n" \
"v_mfma_f32_16x16x32_bf16 v[80:83], v[220:223], v[104:107], v[80:83]\n" \
"ds_read_b64_tr_b16 v[104:105], v230 offset:40960\n" \
"ds_read_b64_tr_b16 v[106:107], v230 offset:45056\n" \
"v_mfma_f32_16x16x32_bf16 v[20:23], v[212:215], v[108:111], v[20:23]\n" \
"v_mfma_f32_16x16x32_bf16 v[52:55], v[216:219], v[108:111], v[52:55]\n" \
"v_mfma_f32_16x16x32_bf16 v[84:87], v[220:223], v[108:111], v[84:87]\n" \
"ds_read_b64_tr_b16 v[108:109], v231 offset:40960\n" \
"ds_read_b64_tr_b16 v[110:111], v231 offset:45056\n" \
"s_waitcnt lgkmcnt(8)\n" \
"v_mfma_f32_16x16x32_bf16 v[24:27], v[152:155], v[112:115], v[24:27]\n" \
"v_mfma_f32_16x16x32_bf16 v[56:59], v[156:159], v[112:115], v[56:59]\n" \
"v_mfma_f32_16x16x32_bf16 v[88:91], v[160:163], v[112:115], v[88:91]\n" \
"ds_read_b64_tr_b16 v[112:113], v230 offset:49152\n" \
"ds_read_b64_tr_b16 v[114:115], v230 offset:53248\n" \
"v_mfma_f32_16x16x32_bf16 v[28:31], v[152:155], v[116:119], v[28:31]\n" \
"v_mfma_f32_16x16x32_bf16 v[60:63], v[156:159], v[116:119], v[60:63]\n" \
"v_cvt_pk_bf16_f32 v128, v12, v13\n" \
"v_cvt_pk_bf16_f32 v129, v14, v15\n" \
"v_mfma_f32_16x16x32_bf16 v[92:95], v[160:163], v[116:119], v[92:95]\n" \
"ds_read_b64_tr_b16 v[116:117], v231 offset:49152\n" \
"ds_read_b64_tr_b16 v[118:119], v231 offset:53248\n" \
"v_mfma_f32_16x16x32_bf16 v[24:27], v[164:167], v[120:123], v[24:27]\n" \
"v_cvt_pk_bf16_f32 v130, v44, v45\n" \
"v_cvt_pk_bf16_f32 v131, v46, v47\n" \
"v_mfma_f32_16x16x32_bf16 v[56:59], v[168:171], v[120:123], v[56:59]\n" \
"v_cvt_pk_bf16_f32 v132, v76, v77\n" \
"v_cvt_pk_bf16_f32 v133, v78, v79\n" \
"v_mfma_f32_16x16x32_bf16 v[88:91], v[172:175], v[120:123], v[88:91]\n" \
"ds_read_b64_tr_b16 v[120:121], v230 offset:57344\n" \
"ds_read_b64_tr_b16 v[122:123], v230 offset:61440\n" \
"v_mfma_f32_16x16x32_bf16 v[28:31], v[164:167], v[124:127], v[28:31]\n" \
"v_min_u32 v128, -2, v128\n" \
"v_min_u32 v129, -2, v129\n" \
"v_mfma_f32_16x16x32_bf16 v[60:63], v[168:171], v[124:127], v[60:63]\n" \
"v_min_u32 v130, -2, v130\n" \
"v_min_u32 v131, -2, v131\n" \
"v_mfma_f32_16x16x32_bf16 v[92:95], v[172:175], v[124:127], v[92:95]\n" \
"ds_read_b64_tr_b16 v[124:125], v231 offset:57344\n" \
"ds_read_b64_tr_b16 v[126:127], v231 offset:61440\n" \
"s_waitcnt lgkmcnt(8)\n" \
"v_mfma_f32_16x16x32_bf16 v[24:27], v[176:179], v[96:99], v[24:27]\n" \
"v_min_u32 v132, -2, v132\n" \
"v_min_u32 v133, -2, v133\n" \
"v_mfma_f32_16x16x32_bf16 v[56:59], v[180:183], v[96:99], v[56:59]\n" \
"buffer_store_dwordx4 v[128:131], v232, s[64:67], s71 offen sc1\n" \
"buffer_store_dwordx2 v[132:133], v233, s[64:67], s71 offen sc1\n" \
"v_mfma_f32_16x16x32_bf16 v[88:91], v[184:187], v[96:99], v[88:91]\n" \
"v_cvt_pk_bf16_f32 v134, v16, v17\n" \
"v_cvt_pk_bf16_f32 v135, v18, v19\n" \
"v_mfma_f32_16x16x32_bf16 v[28:31], v[176:179], v[100:103], v[28:31]\n" \
"v_cvt_pk_bf16_f32 v136, v48, v49\n" \
"v_cvt_pk_bf16_f32 v137, v50, v51\n" \
"v_mfma_f32_16x16x32_bf16 v[60:63], v[180:183], v[100:103], v[60:63]\n" \
"v_cvt_pk_bf16_f32 v138, v80, v81\n" \
"v_cvt_pk_bf16_f32 v139, v82, v83\n" \
"v_mfma_f32_16x16x32_bf16 v[92:95], v[184:187], v[100:103], v[92:95]\n" \
"v_min_u32 v134, -2, v134\n" \
"v_min_u32 v135, -2, v135\n" \
"v_mfma_f32_16x16x32_bf16 v[24:27], v[188:191], v[104:107], v[24:27]\n" \
"v_min_u32 v136, -2, v136\n" \
"v_min_u32 v137, -2, v137\n" \
"v_mfma_f32_16x16x32_bf16 v[56:59], v[192:195], v[104:107], v[56:59]\n" \
"v_min_u32 v138, -2, v138\n" \
"v_min_u32 v139, -2, v139\n" \
"v_mfma_f32_16x16x32_bf16 v[88:91], v[196:199], v[104:107], v[88:91]\n" \
"buffer_store_dwordx4 v[134:137], v232, s[64:67], s72 offen sc1\n" \
"buffer_store_dwordx2 v[138:139], v233, s[64:67], s72 offen sc1\n" \
"v_mfma_f32_16x16x32_bf16 v[28:31], v[188:191], v[108:111], v[28:31]\n" \
"v_cvt_pk_bf16_f32 v140, v20, v21\n" \
"v_cvt_pk_bf16_f32 v141, v22, v23\n" \
"v_mfma_f32_16x16x32_bf16 v[60:63], v[192:195], v[108:111], v[60:63]\n" \
"v_cvt_pk_bf16_f32 v142, v52, v53\n" \
"v_cvt_pk_bf16_f32 v143, v54, v55\n" \
"v_mfma_f32_16x16x32_bf16 v[92:95], v[196:199], v[108:111], v[92:95]\n" \
"v_cvt_pk_bf16_f32 v144, v84, v85\n" \
"v_cvt_pk_bf16_f32 v145, v86, v87\n" \
"s_waitcnt lgkmcnt(0)\n" \
"v_mfma_f32_16x16x32_bf16 v[24:27], v[200:203], v[112:115], v[24:27]\n" \
"v_min_u32 v140, -2, v140\n" \
"v_min_u32 v141, -2, v141\n" \
"v_mfma_f32_16x16x32_bf16 v[56:59], v[204:207], v[112:115], v[56:59]\n" \
"v_min_u32 v142, -2, v142\n" \
"v_min_u32 v143, -2, v143\n" \
"v_mfma_f32_16x16x32_bf16 v[88:91], v[208:211], v[112:115], v[88:91]\n" \
"v_min_u32 v144, -2, v144\n" \
"v_min_u32 v145, -2, v145\n" \
"v_mfma_f32_16x16x32_bf16 v[28:31], v[200:203], v[116:119], v[28:31]\n" \
"buffer_store_dwordx4 v[140:143], v232, s[64:67], s73 offen sc1\n" \
"buffer_store_dwordx2 v[144:145], v233, s[64:67], s73 offen sc1\n" \
"v_mfma_f32_16x16x32_bf16 v[60:63], v[204:207], v[116:119], v[60:63]\n" \
"v_mfma_f32_16x16x32_bf16 v[92:95], v[208:211], v[116:119], v[92:95]\n" \
"v_mfma_f32_16x16x32_bf16 v[24:27], v[212:215], v[120:123], v[24:27]\n" \
"v_mfma_f32_16x16x32_bf16 v[56:59], v[216:219], v[120:123], v[56:59]\n" \
"v_mfma_f32_16x16x32_bf16 v[88:91], v[220:223], v[120:123], v[88:91]\n" \
"v_mfma_f32_16x16x32_bf16 v[28:31], v[212:215], v[124:127], v[28:31]\n" \
"v_mfma_f32_16x16x32_bf16 v[60:63], v[216:219], v[124:127], v[60:63]\n" \
"v_mfma_f32_16x16x32_bf16 v[92:95], v[220:223], v[124:127], v[92:95]\n" \
"s_nop 7\n"
#define GEMM_CLOB "v96","v97","v98","v99","v100","v101","v102","v103","v104","v105","v106","v107","v108","v109","v110","v111","v112","v113","v114","v115","v116","v117","v118","v119","v120","v121","v122","v123","v124","v125","v126","v127","v128","v129","v130","v131","v132","v133","v134","v135","v136","v137","v138","v139","v140","v141","v142","v143","v144","v145","v146","v147","v148","v149","v150","v151","v152","v153","v154","v155","v156","v157","v158","v159","v160","v161","v162","v163","v164","v165","v166","v167","v168","v169","v170","v171","v172","v173","v174","v175","v176","v177","v178","v179","v180","v181","v182","v183","v184","v185","v186","v187","v188","v189","v190","v191","v192","v193","v194","v195","v196","v197","v198","v199","v200","v201","v202","v203","v204","v205","v206","v207","v208","v209","v210","v211","v212","v213","v214","v215","v216","v217","v218","v219","v220","v221","v222","v223","s59","s60","scc","memory"

#define P1_ASM_QK \
"s_mov_b32 s58, 0\n" \
"s_mov_b32 s59, 0\n" \
"s_mov_b32 s61, 0\n" \
"s_mov_b32 s60, 0\n" \
"s_mov_b32 s63, 34816\n" \
"s_add_u32 m0, s56, s61\n" \
"s_nop 0\n" \
"buffer_load_dwordx4 v136, s[40:43], s58 offen offset:0 lds\n" \
"v_mov_b32 v71, 0\n" \
"v_mov_b32 v70, 0\n" \
"v_mov_b32 v69, 0\n" \
"v_mov_b32 v68, 0\n" \
"buffer_load_dwordx4 v137, s[40:43], s58 offen offset:1024 lds\n" \
"v_mov_b32 v67, 0\n" \
"v_mov_b32 v66, 0\n" \
"v_mov_b32 v65, 0\n" \
"v_mov_b32 v64, 0\n" \
"buffer_load_dwordx4 v138, s[40:43], s58 offen offset:2048 lds\n" \
"v_mov_b32 v63, 0\n" \
"v_mov_b32 v62, 0\n" \
"v_mov_b32 v61, 0\n" \
"v_mov_b32 v60, 0\n" \
"buffer_load_dwordx4 v139, s[40:43], s58 offen offset:3072 lds\n" \
"v_mov_b32 v59, 0\n" \
"v_mov_b32 v58, 0\n" \
"v_mov_b32 v57, 0\n" \
"v_mov_b32 v56, 0\n" \
"s_add_u32 s58, s58, 128\n" \
"s_add_u32 m0, s57, s61\n" \
"s_nop 0\n" \
"buffer_load_dwordx4 v140, s[44:47], s59 offen offset:0 lds\n" \
"v_mov_b32 v55, 0\n" \
"v_mov_b32 v54, 0\n" \
"v_mov_b32 v53, 0\n" \
"v_mov_b32 v52, 0\n" \
"buffer_load_dwordx4 v141, s[44:47], s59 offen offset:1024 lds\n" \
"v_mov_b32 v51, 0\n" \
"v_mov_b32 v50, 0\n" \
"v_mov_b32 v49, 0\n" \
"v_mov_b32 v48, 0\n" \
"buffer_load_dwordx4 v142, s[44:47], s59 offen offset:2048 lds\n" \
"v_mov_b32 v47, 0\n" \
"v_mov_b32 v46, 0\n" \
"v_mov_b32 v45, 0\n" \
"v_mov_b32 v44, 0\n" \
"buffer_load_dwordx4 v143, s[44:47], s59 offen offset:3072 lds\n" \
"v_mov_b32 v43, 0\n" \
"v_mov_b32 v42, 0\n" \
"v_mov_b32 v41, 0\n" \
"v_mov_b32 v40, 0\n" \
"s_add_u32 m0, m0, 1024\n" \
"s_mov_b32 exec_hi, 0\n" \
"buffer_load_dwordx4 v144, s[44:47], s59 offen offset:3072 lds\n" \
"s_mov_b32 exec_hi, -1\n" \
"v_mov_b32 v39, 0\n" \
"v_mov_b32 v38, 0\n" \
"v_mov_b32 v37, 0\n" \
"v_mov_b32 v36, 0\n" \
"s_add_u32 s59, s59, 221184\n" \
"s_add_u32 s61, s61, s63\n" \
"s_cmp_eq_u32 s61, 139264\n" \
"s_cselect_b32 s61, 0, s61\n" \
"s_add_u32 m0, s56, s61\n" \
"s_nop 0\n" \
"buffer_load_dwordx4 v136, s[40:43], s58 offen offset:0 lds\n" \
"v_mov_b32 v35, 0\n" \
"v_mov_b32 v34, 0\n" \
"v_mov_b32 v33, 0\n" \
"v_mov_b32 v32, 0\n" \
"buffer_load_dwordx4 v137, s[40:43], s58 offen offset:1024 lds\n" \
"v_mov_b32 v31, 0\n" \
"v_mov_b32 v30, 0\n" \
"v_mov_b32 v29, 0\n" \
"v_mov_b32 v28, 0\n" \
"buffer_load_dwordx4 v138, s[40:43], s58 offen offset:2048 lds\n" \
"v_mov_b32 v27, 0\n" \
"v_mov_b32 v26, 0\n" \
"v_mov_b32 v25, 0\n" \
"v_mov_b32 v24, 0\n" \
"buffer_load_dwordx4 v139, s[40:43], s58 offen offset:3072 lds\n" \
"v_mov_b32 v23, 0\n" \
"v_mov_b32 v22, 0\n" \
"v_mov_b32 v21, 0\n" \
"v_mov_b32 v20, 0\n" \
"s_add_u32 s58, s58, 128\n" \
"s_add_u32 m0, s57, s61\n" \
"s_nop 0\n" \
"buffer_load_dwordx4 v140, s[44:47], s59 offen offset:0 lds\n" \
"v_mov_b32 v19, 0\n" \
"v_mov_b32 v18, 0\n" \
"v_mov_b32 v17, 0\n" \
"v_mov_b32 v16, 0\n" \
"buffer_load_dwordx4 v141, s[44:47], s59 offen offset:1024 lds\n" \
"v_mov_b32 v15, 0\n" \
"v_mov_b32 v14, 0\n" \
"v_mov_b32 v13, 0\n" \
"v_mov_b32 v12, 0\n" \
"buffer_load_dwordx4 v142, s[44:47], s59 offen offset:2048 lds\n" \
"v_mov_b32 v11, 0\n" \
"v_mov_b32 v10, 0\n" \
"v_mov_b32 v9, 0\n" \
"v_mov_b32 v8, 0\n" \
"buffer_load_dwordx4 v143, s[44:47], s59 offen offset:3072 lds\n" \
"v_mov_b32 v7, 0\n" \
"v_mov_b32 v6, 0\n" \
"v_mov_b32 v5, 0\n" \
"v_mov_b32 v4, 0\n" \
"s_add_u32 m0, m0, 1024\n" \
"s_mov_b32 exec_hi, 0\n" \
"buffer_load_dwordx4 v144, s[44:47], s59 offen offset:3072 lds\n" \
"s_mov_b32 exec_hi, -1\n" \
"v_mov_b32 v3, 0\n" \
"v_mov_b32 v2, 0\n" \
"v_mov_b32 v1, 0\n" \
"v_mov_b32 v0, 0\n" \
"s_add_u32 s59, s59, 221184\n" \
"s_add_u32 s61, s61, s63\n" \
"s_cmp_eq_u32 s61, 139264\n" \
"s_cselect_b32 s61, 0, s61\n" \
"s_add_u32 m0, s56, s61\n" \
"s_nop 0\n" \
"buffer_load_dwordx4 v136, s[40:43], s58 offen offset:0 lds\n" \
"buffer_load_dwordx4 v137, s[40:43], s58 offen offset:1024 lds\n" \
"buffer_load_dwordx4 v138, s[40:43], s58 offen offset:2048 lds\n" \
"buffer_load_dwordx4 v139, s[40:43], s58 offen offset:3072 lds\n" \
"s_add_u32 s58, s58, 128\n" \
"s_waitcnt vmcnt(13)\n" \
"s_barrier\n" \
"ds_read_b128 v[108:111], v134 offset:0\n" \
"ds_read_b128 v[112:115], v134 offset:2048\n" \
"ds_read_b64_tr_b16 v[72:73], v124 offset:0\n" \
"ds_read_b64_tr_b16 v[74:75], v124 offset:2304\n" \
"ds_read_b64_tr_b16 v[76:77], v125 offset:0\n" \
"ds_read_b64_tr_b16 v[78:79], v125 offset:2304\n" \
"ds_read_b64_tr_b16 v[80:81], v126 offset:0\n" \
"ds_read_b64_tr_b16 v[82:83], v126 offset:2304\n" \
"ds_read_b64_tr_b16 v[84:85], v127 offset:0\n" \
"ds_read_b64_tr_b16 v[86:87], v127 offset:2304\n" \
"ds_read_b64_tr_b16 v[88:89], v128 offset:0\n" \
"ds_read_b64_tr_b16 v[90:91], v128 offset:2304\n" \
"ds_read_b64_tr_b16 v[92:93], v129 offset:0\n" \
"ds_read_b64_tr_b16 v[94:95], v129 offset:2304\n" \
"ds_read_b64_tr_b16 v[96:97], v130 offset:0\n" \
"ds_read_b64_tr_b16 v[98:99], v130 offset:2304\n" \
"ds_read_b64_tr_b16 v[100:101], v131 offset:0\n" \
"ds_read_b64_tr_b16 v[102:103], v131 offset:2304\n" \
"ds_read_b64_tr_b16 v[104:105], v132 offset:0\n" \
"s_waitcnt lgkmcnt(0)\n" \
"Lp1q_%=_loop:\n" \
"s_cmp_eq_u32 s61, 34816\n" \
"s_waitcnt lgkmcnt(11)\n" \
"v_mfma_f32_16x16x32_bf16 v[0:3], v[72:75], v[108:111], v[0:3]\n" \
"ds_read_b64_tr_b16 v[106:107], v132 offset:2304\n" \
"s_cselect_b32 s62, 4294862848, s63\n" \
"v_mfma_f32_16x16x32_bf16 v[4:7], v[72:75], v[112:115], v[4:7]\n" \
"ds_read_b64_tr_b16 v[72:73], v124 offset:9216\n" \
"ds_read_b128 v[116:119], v135 offset:0\n" \
"v_mfma_f32_16x16x32_bf16 v[8:11], v[76:79], v[108:111], v[8:11]\n" \
"ds_read_b64_tr_b16 v[74:75], v124 offset:11520\n" \
"v_add_u32 v124, s62, v124\n" \
"v_mfma_f32_16x16x32_bf16 v[12:15], v[76:79], v[112:115], v[12:15]\n" \
"ds_read_b64_tr_b16 v[76:77], v125 offset:9216\n" \
"ds_read_b128 v[120:123], v135 offset:2048\n" \
"v_mfma_f32_16x16x32_bf16 v[16:19], v[80:83], v[108:111], v[16:19]\n" \
"ds_read_b64_tr_b16 v[78:79], v125 offset:11520\n" \
"v_add_u32 v125, s62, v125\n" \
"s_add_u32 m0, s57, s61\n" \
"v_mfma_f32_16x16x32_bf16 v[20:23], v[80:83], v[112:115], v[20:23]\n" \
"ds_read_b64_tr_b16 v[80:81], v126 offset:9216\n" \
"buffer_load_dwordx4 v140, s[44:47], s59 offen offset:0 lds\n" \
"s_waitcnt lgkmcnt(13)\n" \
"v_mfma_f32_16x16x32_bf16 v[24:27], v[84:87], v[108:111], v[24:27]\n" \
"ds_read_b64_tr_b16 v[82:83], v126 offset:11520\n" \
"v_add_u32 v126, s62, v126\n" \
"v_mfma_f32_16x16x32_bf16 v[28:31], v[84:87], v[112:115], v[28:31]\n" \
"ds_read_b64_tr_b16 v[84:85], v127 offset:9216\n" \
"buffer_load_dwordx4 v141, s[44:47], s59 offen offset:1024 lds\n" \
"v_mfma_f32_16x16x32_bf16 v[32:35], v[88:91], v[108:111], v[32:35]\n" \
"ds_read_b64_tr_b16 v[86:87], v127 offset:11520\n" \
"v_add_u32 v127, s62, v127\n" \
"v_mfma_f32_16x16x32_bf16 v[36:39], v[88:91], v[112:115], v[36:39]\n" \
"ds_read_b64_tr_b16 v[88:89], v128 offset:9216\n" \
"buffer_load_dwordx4 v142, s[44:47], s59 offen offset:2048 lds\n" \
"v_mfma_f32_16x16x32_bf16 v[40:43], v[92:95], v[108:111], v[40:43]\n" \
"ds_read_b64_tr_b16 v[90:91], v128 offset:11520\n" \
"v_add_u32 v128, s62, v128\n" \
"v_mfma_f32_16x16x32_bf16 v[44:47], v[92:95], v[112:115], v[44:47]\n" \
"ds_read_b64_tr_b16 v[92:93], v129 offset:9216\n" \
"s_waitcnt lgkmcnt(13)\n" \
"v_mfma_f32_16x16x32_bf16 v[48:51], v[96:99], v[108:111], v[48:51]\n" \
"ds_read_b64_tr_b16 v[94:95], v129 offset:11520\n" \
"v_add_u32 v129, s62, v129\n" \
"v_mfma_f32_16x16x32_bf16 v[52:55], v[96:99], v[112:115], v[52:55]\n" \
"ds_read_b64_tr_b16 v[96:97], v130 offset:9216\n" \
"buffer_load_dwordx4 v143, s[44:47], s59 offen offset:3072 lds\n" \
"v_mfma_f32_16x16x32_bf16 v[56:59], v[100:103], v[108:111], v[56:59]\n" \
"ds_read_b64_tr_b16 v[98:99], v130 offset:11520\n" \
"v_add_u32 v130, s62, v130\n" \
"v_mfma_f32_16x16x32_bf16 v[60:63], v[100:103], v[112:115], v[60:63]\n" \
"ds_read_b64_tr_b16 v[100:101], v131 offset:9216\n" \
"s_add_u32 m0, m0, 1024\n" \
"v_mfma_f32_16x16x32_bf16 v[64:67], v[104:107], v[108:111], v[64:67]\n" \
"ds_read_b64_tr_b16 v[102:103], v131 offset:11520\n" \
"v_add_u32 v131, s62, v131\n" \
"v_mfma_f32_16x16x32_bf16 v[68:71], v[104:107], v[112:115], v[68:71]\n" \
"ds_read_b64_tr_b16 v[104:105], v132 offset:9216\n" \
"s_mov_b32 exec_hi, 0\n" \
"buffer_load_dwordx4 v144, s[44:47], s59 offen offset:3072 lds\n" \
"s_mov_b32 exec_hi, -1\n" \
"v_add_u32 v134, s62, v134\n" \
"s_waitcnt vmcnt(9)\n" \
"s_barrier\n" \
"s_waitcnt lgkmcnt(11)\n" \
"v_mfma_f32_16x16x32_bf16 v[0:3], v[72:75], v[116:119], v[0:3]\n" \
"ds_read_b64_tr_b16 v[106:107], v132 offset:11520\n" \
"v_add_u32 v132, s62, v132\n" \
"v_mfma_f32_16x16x32_bf16 v[4:7], v[72:75], v[120:123], v[4:7]\n" \
"ds_read_b64_tr_b16 v[72:73], v124 offset:0\n" \
"ds_read_b128 v[108:111], v134 offset:0\n" \
"v_mfma_f32_16x16x32_bf16 v[8:11], v[76:79], v[116:119], v[8:11]\n" \
"ds_read_b64_tr_b16 v[74:75], v124 offset:2304\n" \
"s_add_u32 s59, s59, 221184\n" \
"v_mfma_f32_16x16x32_bf16 v[12:15], v[76:79], v[120:123], v[12:15]\n" \
"ds_read_b64_tr_b16 v[76:77], v125 offset:0\n" \
"ds_read_b128 v[112:115], v134 offset:2048\n" \
"v_mfma_f32_16x16x32_bf16 v[16:19], v[80:83], v[116:119], v[16:19]\n" \
"ds_read_b64_tr_b16 v[78:79], v125 offset:2304\n" \
"s_add_u32 s61, s61, s63\n" \
"v_mfma_f32_16x16x32_bf16 v[20:23], v[80:83], v[120:123], v[20:23]\n" \
"ds_read_b64_tr_b16 v[80:81], v126 offset:0\n" \
"s_cmp_eq_u32 s61, 139264\n" \
"s_waitcnt lgkmcnt(13)\n" \
"v_mfma_f32_16x16x32_bf16 v[24:27], v[84:87], v[116:119], v[24:27]\n" \
"ds_read_b64_tr_b16 v[82:83], v126 offset:2304\n" \
"s_cselect_b32 s61, 0, s61\n" \
"s_add_u32 m0, s56, s61\n" \
"v_mfma_f32_16x16x32_bf16 v[28:31], v[84:87], v[120:123], v[28:31]\n" \
"ds_read_b64_tr_b16 v[84:85], v127 offset:0\n" \
"buffer_load_dwordx4 v136, s[40:43], s58 offen offset:0 lds\n" \
"v_mfma_f32_16x16x32_bf16 v[32:35], v[88:91], v[116:119], v[32:35]\n" \
"ds_read_b64_tr_b16 v[86:87], v127 offset:2304\n" \
"v_mfma_f32_16x16x32_bf16 v[36:39], v[88:91], v[120:123], v[36:39]\n" \
"ds_read_b64_tr_b16 v[88:89], v128 offset:0\n" \
"buffer_load_dwordx4 v137, s[40:43], s58 offen offset:1024 lds\n" \
"v_mfma_f32_16x16x32_bf16 v[40:43], v[92:95], v[116:119], v[40:43]\n" \
"ds_read_b64_tr_b16 v[90:91], v128 offset:2304\n" \
"v_add_u32 v135, s62, v135\n" \
"v_mfma_f32_16x16x32_bf16 v[44:47], v[92:95], v[120:123], v[44:47]\n" \
"ds_read_b64_tr_b16 v[92:93], v129 offset:0\n" \
"s_waitcnt lgkmcnt(13)\n" \
"v_mfma_f32_16x16x32_bf16 v[48:51], v[96:99], v[116:119], v[48:51]\n" \
"ds_read_b64_tr_b16 v[94:95], v129 offset:2304\n" \
"buffer_load_dwordx4 v138, s[40:43], s58 offen offset:2048 lds\n" \
"v_mfma_f32_16x16x32_bf16 v[52:55], v[96:99], v[120:123], v[52:55]\n" \
"ds_read_b64_tr_b16 v[96:97], v130 offset:0\n" \
"s_add_u32 s60, s60, 1\n" \
"v_mfma_f32_16x16x32_bf16 v[56:59], v[100:103], v[116:119], v[56:59]\n" \
"ds_read_b64_tr_b16 v[98:99], v130 offset:2304\n" \
"buffer_load_dwordx4 v139, s[40:43], s58 offen offset:3072 lds\n" \
"v_mfma_f32_16x16x32_bf16 v[60:63], v[100:103], v[120:123], v[60:63]\n" \
"ds_read_b64_tr_b16 v[100:101], v131 offset:0\n" \
"s_add_u32 s58, s58, 128\n" \
"v_mfma_f32_16x16x32_bf16 v[64:67], v[104:107], v[116:119], v[64:67]\n" \
"ds_read_b64_tr_b16 v[102:103], v131 offset:2304\n" \
"s_cmp_eq_u32 s58, 1152\n" \
"s_cselect_b32 s58, 0x40000000, s58\n" \
"v_mfma_f32_16x16x32_bf16 v[68:71], v[104:107], v[120:123], v[68:71]\n" \
"ds_read_b64_tr_b16 v[104:105], v132 offset:0\n" \
"s_cmp_eq_u32 s60, 9\n" \
"s_cbranch_scc0 Lp1q_%=_loop\n" \
"s_waitcnt vmcnt(0) lgkmcnt(0)\n" \
"s_nop 7\n"
#define P1_ASM_V \
"s_mov_b32 s58, 0\n" \
"s_mov_b32 s59, 0\n" \
"s_mov_b32 s61, 0\n" \
"s_mov_b32 s60, 0\n" \
"s_mov_b32 s63, 34816\n" \
"s_add_u32 m0, s56, s61\n" \
"s_nop 0\n" \
"buffer_load_dwordx4 v136, s[40:43], s58 offen offset:0 lds\n" \
"v_mov_b32 v71, 0\n" \
"v_mov_b32 v70, 0\n" \
"v_mov_b32 v69, 0\n" \
"v_mov_b32 v68, 0\n" \
"buffer_load_dwordx4 v137, s[40:43], s58 offen offset:1024 lds\n" \
"v_mov_b32 v67, 0\n" \
"v_mov_b32 v66, 0\n" \
"v_mov_b32 v65, 0\n" \
"v_mov_b32 v64, 0\n" \
"buffer_load_dwordx4 v138, s[40:43], s58 offen offset:2048 lds\n" \
"v_mov_b32 v63, 0\n" \
"v_mov_b32 v62, 0\n" \
"v_mov_b32 v61, 0\n" \
"v_mov_b32 v60, 0\n" \
"buffer_load_dwordx4 v139, s[40:43], s58 offen offset:3072 lds\n" \
"v_mov_b32 v59, 0\n" \
"v_mov_b32 v58, 0\n" \
"v_mov_b32 v57, 0\n" \
"v_mov_b32 v56, 0\n" \
"s_add_u32 s58, s58, 128\n" \
"s_add_u32 m0, s57, s61\n" \
"s_nop 0\n" \
"buffer_load_dwordx4 v140, s[44:47], s59 offen offset:0 lds\n" \
"v_mov_b32 v55, 0\n" \
"v_mov_b32 v54, 0\n" \
"v_mov_b32 v53, 0\n" \
"v_mov_b32 v52, 0\n" \
"buffer_load_dwordx4 v141, s[44:47], s59 offen offset:1024 lds\n" \
"v_mov_b32 v51, 0\n" \
"v_mov_b32 v50, 0\n" \
"v_mov_b32 v49, 0\n" \
"v_mov_b32 v48, 0\n" \
"buffer_load_dwordx4 v142, s[44:47], s59 offen offset:2048 lds\n" \
"v_mov_b32 v47, 0\n" \
"v_mov_b32 v46, 0\n" \
"v_mov_b32 v45, 0\n" \
"v_mov_b32 v44, 0\n" \
"buffer_load_dwordx4 v143, s[44:47], s59 offen offset:3072 lds\n" \
"v_mov_b32 v43, 0\n" \
"v_mov_b32 v42, 0\n" \
"v_mov_b32 v41, 0\n" \
"v_mov_b32 v40, 0\n" \
"s_add_u32 m0, m0, 1024\n" \
"s_mov_b32 exec_hi, 0\n" \
"buffer_load_dwordx4 v144, s[44:47], s59 offen offset:3072 lds\n" \
"s_mov_b32 exec_hi, -1\n" \
"v_mov_b32 v39, 0\n" \
"v_mov_b32 v38, 0\n" \
"v_mov_b32 v37, 0\n" \
"v_mov_b32 v36, 0\n" \
"s_add_u32 s59, s59, 221184\n" \
"s_add_u32 s61, s61, s63\n" \
"s_cmp_eq_u32 s61, 139264\n" \
"s_cselect_b32 s61, 0, s61\n" \
"s_add_u32 m0, s56, s61\n" \
"s_nop 0\n" \
"buffer_load_dwordx4 v136, s[40:43], s58 offen offset:0 lds\n" \
"v_mov_b32 v35, 0\n" \
"v_mov_b32 v34, 0\n" \
"v_mov_b32 v33, 0\n" \
"v_mov_b32 v32, 0\n" \
"buffer_load_dwordx4 v137, s[40:43], s58 offen offset:1024 lds\n" \
"v_mov_b32 v31, 0\n" \
"v_mov_b32 v30, 0\n" \
"v_mov_b32 v29, 0\n" \
"v_mov_b32 v28, 0\n" \
"buffer_load_dwordx4 v138, s[40:43], s58 offen offset:2048 lds\n" \
"v_mov_b32 v27, 0\n" \
"v_mov_b32 v26, 0\n" \
"v_mov_b32 v25, 0\n" \
"v_mov_b32 v24, 0\n" \
"buffer_load_dwordx4 v139, s[40:43], s58 offen offset:3072 lds\n" \
"v_mov_b32 v23, 0\n" \
"v_mov_b32 v22, 0\n" \
"v_mov_b32 v21, 0\n" \
"v_mov_b32 v20, 0\n" \
"s_add_u32 s58, s58, 128\n" \
"s_add_u32 m0, s57, s61\n" \
"s_nop 0\n" \
"buffer_load_dwordx4 v140, s[44:47], s59 offen offset:0 lds\n" \
"v_mov_b32 v19, 0\n" \
"v_mov_b32 v18, 0\n" \
"v_mov_b32 v17, 0\n" \
"v_mov_b32 v16, 0\n" \
"buffer_load_dwordx4 v141, s[44:47], s59 offen offset:1024 lds\n" \
"v_mov_b32 v15, 0\n" \
"v_mov_b32 v14, 0\n" \
"v_mov_b32 v13, 0\n" \
"v_mov_b32 v12, 0\n" \
"buffer_load_dwordx4 v142, s[44:47], s59 offen offset:2048 lds\n" \
"v_mov_b32 v11, 0\n" \
"v_mov_b32 v10, 0\n" \
"v_mov_b32 v9, 0\n" \
"v_mov_b32 v8, 0\n" \
"buffer_load_dwordx4 v143, s[44:47], s59 offen offset:3072 lds\n" \
"v_mov_b32 v7, 0\n" \
"v_mov_b32 v6, 0\n" \
"v_mov_b32 v5, 0\n" \
"v_mov_b32 v4, 0\n" \
"s_add_u32 m0, m0, 1024\n" \
"s_mov_b32 exec_hi, 0\n" \
"buffer_load_dwordx4 v144, s[44:47], s59 offen offset:3072 lds\n" \
"s_mov_b32 exec_hi, -1\n" \
"v_mov_b32 v3, 0\n" \
"v_mov_b32 v2, 0\n" \
"v_mov_b32 v1, 0\n" \
"v_mov_b32 v0, 0\n" \
"s_add_u32 s59, s59, 221184\n" \
"s_add_u32 s61, s61, s63\n" \
"s_cmp_eq_u32 s61, 139264\n" \
"s_cselect_b32 s61, 0, s61\n" \
"s_add_u32 m0, s56, s61\n" \
"s_nop 0\n" \
"buffer_load_dwordx4 v136, s[40:43], s58 offen offset:0 lds\n" \
"buffer_load_dwordx4 v137, s[40:43], s58 offen offset:1024 lds\n" \
"buffer_load_dwordx4 v138, s[40:43], s58 offen offset:2048 lds\n" \
"buffer_load_dwordx4 v139, s[40:43], s58 offen offset:3072 lds\n" \
"s_add_u32 s58, s58, 128\n" \
"s_waitcnt vmcnt(13)\n" \
"s_barrier\n" \
"ds_read_b128 v[108:111], v134 offset:0\n" \
"ds_read_b128 v[112:115], v134 offset:2048\n" \
"ds_read_b64_tr_b16 v[72:73], v124 offset:0\n" \
"ds_read_b64_tr_b16 v[74:75], v124 offset:2304\n" \
"ds_read_b64_tr_b16 v[76:77], v125 offset:0\n" \
"ds_read_b64_tr_b16 v[78:79], v125 offset:2304\n" \
"ds_read_b64_tr_b16 v[80:81], v126 offset:0\n" \
"ds_read_b64_tr_b16 v[82:83], v126 offset:2304\n" \
"ds_read_b64_tr_b16 v[84:85], v127 offset:0\n" \
"ds_read_b64_tr_b16 v[86:87], v127 offset:2304\n" \
"ds_read_b64_tr_b16 v[88:89], v128 offset:0\n" \
"ds_read_b64_tr_b16 v[90:91], v128 offset:2304\n" \
"ds_read_b64_tr_b16 v[92:93], v129 offset:0\n" \
"ds_read_b64_tr_b16 v[94:95], v129 offset:2304\n" \
"ds_read_b64_tr_b16 v[96:97], v130 offset:0\n" \
"ds_read_b64_tr_b16 v[98:99], v130 offset:2304\n" \
"ds_read_b64_tr_b16 v[100:101], v131 offset:0\n" \
"ds_read_b64_tr_b16 v[102:103], v131 offset:2304\n" \
"ds_read_b64_tr_b16 v[104:105], v132 offset:0\n" \
"s_waitcnt lgkmcnt(0)\n" \
"Lp1v_%=_loop:\n" \
"s_cmp_eq_u32 s61, 34816\n" \
"s_waitcnt lgkmcnt(11)\n" \
"v_mfma_f32_16x16x32_bf16 v[0:3], v[108:111], v[72:75], v[0:3]\n" \
"ds_read_b64_tr_b16 v[106:107], v132 offset:2304\n" \
"s_cselect_b32 s62, 4294862848, s63\n" \
"v_mfma_f32_16x16x32_bf16 v[4:7], v[112:115], v[72:75], v[4:7]\n" \
"ds_read_b64_tr_b16 v[72:73], v124 offset:9216\n" \
"ds_read_b128 v[116:119], v135 offset:0\n" \
"v_mfma_f32_16x16x32_bf16 v[8:11], v[108:111], v[76:79], v[8:11]\n" \
"ds_read_b64_tr_b16 v[74:75], v124 offset:11520\n" \
"v_add_u32 v124, s62, v124\n" \
"v_mfma_f32_16x16x32_bf16 v[12:15], v[112:115], v[76:79], v[12:15]\n" \
"ds_read_b64_tr_b16 v[76:77], v125 offset:9216\n" \
"ds_read_b128 v[120:123], v135 offset:2048\n" \
"v_mfma_f32_16x16x32_bf16 v[16:19], v[108:111], v[80:83], v[16:19]\n" \
"ds_read_b64_tr_b16 v[78:79], v125 offset:11520\n" \
"v_add_u32 v125, s62, v125\n" \
"s_add_u32 m0, s57, s61\n" \
"v_mfma_f32_16x16x32_bf16 v[20:23], v[112:115], v[80:83], v[20:23]\n" \
"ds_read_b64_tr_b16 v[80:81], v126 offset:9216\n" \
"buffer_load_dwordx4 v140, s[44:47], s59 offen offset:0 lds\n" \
"s_waitcnt lgkmcnt(13)\n" \
"v_mfma_f32_16x16x32_bf16 v[24:27], v[108:111], v[84:87], v[24:27]\n" \
"ds_read_b64_tr_b16 v[82:83], v126 offset:11520\n" \
"v_add_u32 v126, s62, v126\n" \
"v_mfma_f32_16x16x32_bf16 v[28:31], v[112:115], v[84:87], v[28:31]\n" \
"ds_read_b64_tr_b16 v[84:85], v127 offset:9216\n" \
"buffer_load_dwordx4 v141, s[44:47], s59 offen offset:1024 lds\n" \
"v_mfma_f32_16x16x32_bf16 v[32:35], v[108:111], v[88:91], v[32:35]\n" \
"ds_read_b64_tr_b16 v[86:87], v127 offset:11520\n" \
"v_add_u32 v127, s62, v127\n" \
"v_mfma_f32_16x16x32_bf16 v[36:39], v[112:115], v[88:91], v[36:39]\n" \
"ds_read_b64_tr_b16 v[88:89], v128 offset:9216\n" \
"buffer_load_dwordx4 v142, s[44:47], s59 offen offset:2048 lds\n" \
"v_mfma_f32_16x16x32_bf16 v[40:43], v[108:111], v[92:95], v[40:43]\n" \
"ds_read_b64_tr_b16 v[90:91], v128 offset:11520\n" \
"v_add_u32 v128, s62, v128\n" \
"v_mfma_f32_16x16x32_bf16 v[44:47], v[112:115], v[92:95], v[44:47]\n" \
"ds_read_b64_tr_b16 v[92:93], v129 offset:9216\n" \
"s_waitcnt lgkmcnt(13)\n" \
"v_mfma_f32_16x16x32_bf16 v[48:51], v[108:111], v[96:99], v[48:51]\n" \
"ds_read_b64_tr_b16 v[94:95], v129 offset:11520\n" \
"v_add_u32 v129, s62, v129\n" \
"v_mfma_f32_16x16x32_bf16 v[52:55], v[112:115], v[96:99], v[52:55]\n" \
"ds_read_b64_tr_b16 v[96:97], v130 offset:9216\n" \
"buffer_load_dwordx4 v143, s[44:47], s59 offen offset:3072 lds\n" \
"v_mfma_f32_16x16x32_bf16 v[56:59], v[108:111], v[100:103], v[56:59]\n" \
"ds_read_b64_tr_b16 v[98:99], v130 offset:11520\n" \
"v_add_u32 v130, s62, v130\n" \
"v_mfma_f32_16x16x32_bf16 v[60:63], v[112:115], v[100:103], v[60:63]\n" \
"ds_read_b64_tr_b16 v[100:101], v131 offset:9216\n" \
"s_add_u32 m0, m0, 1024\n" \
"v_mfma_f32_16x16x32_bf16 v[64:67], v[108:111], v[104:107], v[64:67]\n" \
"ds_read_b64_tr_b16 v[102:103], v131 offset:11520\n" \
"v_add_u32 v131, s62, v131\n" \
"v_mfma_f32_16x16x32_bf16 v[68:71], v[112:115], v[104:107], v[68:71]\n" \
"ds_read_b64_tr_b16 v[104:105], v132 offset:9216\n" \
"s_mov_b32 exec_hi, 0\n" \
"buffer_load_dwordx4 v144, s[44:47], s59 offen offset:3072 lds\n" \
"s_mov_b32 exec_hi, -1\n" \
"v_add_u32 v134, s62, v134\n" \
"s_waitcnt vmcnt(9)\n" \
"s_barrier\n" \
"s_waitcnt lgkmcnt(11)\n" \
"v_mfma_f32_16x16x32_bf16 v[0:3], v[116:119], v[72:75], v[0:3]\n" \
"ds_read_b64_tr_b16 v[106:107], v132 offset:11520\n" \
"v_add_u32 v132, s62, v132\n" \
"v_mfma_f32_16x16x32_bf16 v[4:7], v[120:123], v[72:75], v[4:7]\n" \
"ds_read_b64_tr_b16 v[72:73], v124 offset:0\n" \
"ds_read_b128 v[108:111], v134 offset:0\n" \
"v_mfma_f32_16x16x32_bf16 v[8:11], v[116:119], v[76:79], v[8:11]\n" \
"ds_read_b64_tr_b16 v[74:75], v124 offset:2304\n" \
"s_add_u32 s59, s59, 221184\n" \
"v_mfma_f32_16x16x32_bf16 v[12:15], v[120:123], v[76:79], v[12:15]\n" \
"ds_read_b64_tr_b16 v[76:77], v125 offset:0\n" \
"ds_read_b128 v[112:115], v134 offset:2048\n" \
"v_mfma_f32_16x16x32_bf16 v[16:19], v[116:119], v[80:83], v[16:19]\n" \
"ds_read_b64_tr_b16 v[78:79], v125 offset:2304\n" \
"s_add_u32 s61, s61, s63\n" \
"v_mfma_f32_16x16x32_bf16 v[20:23], v[120:123], v[80:83], v[20:23]\n" \
"ds_read_b64_tr_b16 v[80:81], v126 offset:0\n" \
"s_cmp_eq_u32 s61, 139264\n" \
"s_waitcnt lgkmcnt(13)\n" \
"v_mfma_f32_16x16x32_bf16 v[24:27], v[116:119], v[84:87], v[24:27]\n" \
"ds_read_b64_tr_b16 v[82:83], v126 offset:2304\n" \
"s_cselect_b32 s61, 0, s61\n" \
"s_add_u32 m0, s56, s61\n" \
"v_mfma_f32_16x16x32_bf16 v[28:31], v[120:123], v[84:87], v[28:31]\n" \
"ds_read_b64_tr_b16 v[84:85], v127 offset:0\n" \
"buffer_load_dwordx4 v136, s[40:43], s58 offen offset:0 lds\n" \
"v_mfma_f32_16x16x32_bf16 v[32:35], v[116:119], v[88:91], v[32:35]\n" \
"ds_read_b64_tr_b16 v[86:87], v127 offset:2304\n" \
"v_mfma_f32_16x16x32_bf16 v[36:39], v[120:123], v[88:91], v[36:39]\n" \
"ds_read_b64_tr_b16 v[88:89], v128 offset:0\n" \
"buffer_load_dwordx4 v137, s[40:43], s58 offen offset:1024 lds\n" \
"v_mfma_f32_16x16x32_bf16 v[40:43], v[116:119], v[92:95], v[40:43]\n" \
"ds_read_b64_tr_b16 v[90:91], v128 offset:2304\n" \
"v_add_u32 v135, s62, v135\n" \
"v_mfma_f32_16x16x32_bf16 v[44:47], v[120:123], v[92:95], v[44:47]\n" \
"ds_read_b64_tr_b16 v[92:93], v129 offset:0\n" \
"s_waitcnt lgkmcnt(13)\n" \
"v_mfma_f32_16x16x32_bf16 v[48:51], v[116:119], v[96:99], v[48:51]\n" \
"ds_read_b64_tr_b16 v[94:95], v129 offset:2304\n" \
"buffer_load_dwordx4 v138, s[40:43], s58 offen offset:2048 lds\n" \
"v_mfma_f32_16x16x32_bf16 v[52:55], v[120:123], v[96:99], v[52:55]\n" \
"ds_read_b64_tr_b16 v[96:97], v130 offset:0\n" \
"s_add_u32 s60, s60, 1\n" \
"v_mfma_f32_16x16x32_bf16 v[56:59], v[116:119], v[100:103], v[56:59]\n" \
"ds_read_b64_tr_b16 v[98:99], v130 offset:2304\n" \
"buffer_load_dwordx4 v139, s[40:43], s58 offen offset:3072 lds\n" \
"v_mfma_f32_16x16x32_bf16 v[60:63], v[120:123], v[100:103], v[60:63]\n" \
"ds_read_b64_tr_b16 v[100:101], v131 offset:0\n" \
"s_add_u32 s58, s58, 128\n" \
"v_mfma_f32_16x16x32_bf16 v[64:67], v[116:119], v[104:107], v[64:67]\n" \
"ds_read_b64_tr_b16 v[102:103], v131 offset:2304\n" \
"s_cmp_eq_u32 s58, 1152\n" \
"s_cselect_b32 s58, 0x40000000, s58\n" \
"v_mfma_f32_16x16x32_bf16 v[68:71], v[120:123], v[104:107], v[68:71]\n" \
"ds_read_b64_tr_b16 v[104:105], v132 offset:0\n" \
"s_cmp_eq_u32 s60, 9\n" \
"s_cbranch_scc0 Lp1v_%=_loop\n" \
"s_waitcnt vmcnt(0) lgkmcnt(0)\n" \
"s_nop 7\n"
#define P1_CLOB "v72","v73","v74","v75","v76","v77","v78","v79","v80","v81","v82","v83","v84","v85","v86","v87","v88","v89","v90","v91","v92","v93","v94","v95","v96","v97","v98","v99","v100","v101","v102","v103","v104","v105","v106","v107","v108","v109","v110","v111","v112","v113","v114","v115","v116","v117","v118","v119","v120","v121","v122","v123","s58","s59","s60","s61","s62","s63","scc","memory"

#define SVC_KV \
"s_barrier\n" \
"s_add_u32 m0, s56, 12288\n" \
"s_mov_b32 s58, 12288\n" \
"buffer_load_dwordx4 v200, s[40:43], s58 offen offset:0 sc1 lds\n" \
"buffer_load_dwordx4 v200, s[40:43], s58 offen offset:1024 sc1 lds\n" \
"buffer_load_dwordx4 v200, s[40:43], s58 offen offset:2048 sc1 lds\n" \
"buffer_load_dwordx4 v200, s[40:43], s58 offen offset:3072 sc1 lds\n" \
"s_add_u32 m0, s56, 90112\n" \
"s_mov_b32 s58, 90112\n" \
"buffer_load_dwordx4 v200, s[40:43], s58 offen offset:0 sc1 lds\n" \
"buffer_load_dwordx4 v200, s[40:43], s58 offen offset:1024 sc1 lds\n" \
"buffer_load_dwordx4 v200, s[40:43], s58 offen offset:2048 sc1 lds\n" \
"buffer_load_dwordx4 v200, s[40:43], s58 offen offset:3072 sc1 lds\n" \
"s_add_u32 m0, s56, 16384\n" \
"s_mov_b32 s58, 16384\n" \
"buffer_load_dwordx4 v200, s[40:43], s58 offen offset:0 sc1 lds\n" \
"buffer_load_dwordx4 v200, s[40:43], s58 offen offset:1024 sc1 lds\n" \
"buffer_load_dwordx4 v200, s[40:43], s58 offen offset:2048 sc1 lds\n" \
"buffer_load_dwordx4 v200, s[40:43], s58 offen offset:3072 sc1 lds\n" \
"s_add_u32 m0, s56, 94208\n" \
"s_mov_b32 s58, 94208\n" \
"buffer_load_dwordx4 v200, s[40:43], s58 offen offset:0 sc1 lds\n" \
"buffer_load_dwordx4 v200, s[40:43], s58 offen offset:1024 sc1 lds\n" \
"buffer_load_dwordx4 v200, s[40:43], s58 offen offset:2048 sc1 lds\n" \
"buffer_load_dwordx4 v200, s[40:43], s58 offen offset:3072 sc1 lds\n" \
"s_add_u32 m0, s56, 20480\n" \
"s_mov_b32 s58, 20480\n" \
"buffer_load_dwordx4 v200, s[40:43], s58 offen offset:0 sc1 lds\n" \
"buffer_load_dwordx4 v200, s[40:43], s58 offen offset:1024 sc1 lds\n" \
"buffer_load_dwordx4 v200, s[40:43], s58 offen offset:2048 sc1 lds\n" \
"buffer_load_dwordx4 v200, s[40:43], s58 offen offset:3072 sc1 lds\n" \
"s_add_u32 m0, s56, 98304\n" \
"s_mov_b32 s58, 98304\n" \
"buffer_load_dwordx4 v200, s[40:43], s58 offen offset:0 sc1 lds\n" \
"buffer_load_dwordx4 v200, s[40:43], s58 offen offset:1024 sc1 lds\n" \
"buffer_load_dwordx4 v200, s[40:43], s58 offen offset:2048 sc1 lds\n" \
"buffer_load_dwordx4 v200, s[40:43], s58 offen offset:3072 sc1 lds\n" \
"s_add_u32 m0, s56, 24576\n" \
"s_mov_b32 s58, 24576\n" \
"buffer_load_dwordx4 v200, s[40:43], s58 offen offset:0 sc1 lds\n" \
"buffer_load_dwordx4 v200, s[40:43], s58 offen offset:1024 sc1 lds\n" \
"buffer_load_dwordx4 v200, s[40:43], s58 offen offset:2048 sc1 lds\n" \
"buffer_load_dwordx4 v200, s[40:43], s58 offen offset:3072 sc1 lds\n" \
"s_add_u32 m0, s56, 102400\n" \
"s_mov_b32 s58, 102400\n" \
"buffer_load_dwordx4 v200, s[40:43], s58 offen offset:0 sc1 lds\n" \
"buffer_load_dwordx4 v200, s[40:43], s58 offen offset:1024 sc1 lds\n" \
"buffer_load_dwordx4 v200, s[40:43], s58 offen offset:2048 sc1 lds\n" \
"buffer_load_dwordx4 v200, s[40:43], s58 offen offset:3072 sc1 lds\n" \
"s_add_u32 m0, s56, 28672\n" \
"s_mov_b32 s58, 28672\n" \
"buffer_load_dwordx4 v200, s[40:43], s58 offen offset:0 sc1 lds\n" \
"buffer_load_dwordx4 v200, s[40:43], s58 offen offset:1024 sc1 lds\n" \
"buffer_load_dwordx4 v200, s[40:43], s58 offen offset:2048 sc1 lds\n" \
"buffer_load_dwordx4 v200, s[40:43], s58 offen offset:3072 sc1 lds\n" \
"s_add_u32 m0, s56, 106496\n" \
"s_mov_b32 s58, 106496\n" \
"buffer_load_dwordx4 v200, s[40:43], s58 offen offset:0 sc1 lds\n" \
"buffer_load_dwordx4 v200, s[40:43], s58 offen offset:1024 sc1 lds\n" \
"buffer_load_dwordx4 v200, s[40:43], s58 offen offset:2048 sc1 lds\n" \
"buffer_load_dwordx4 v200, s[40:43], s58 offen offset:3072 sc1 lds\n" \
"s_add_u32 m0, s56, 32768\n" \
"s_mov_b32 s58, 32768\n" \
"buffer_load_dwordx4 v200, s[40:43], s58 offen offset:0 sc1 lds\n" \
"buffer_load_dwordx4 v200, s[40:43], s58 offen offset:1024 sc1 lds\n" \
"buffer_load_dwordx4 v200, s[40:43], s58 offen offset:2048 sc1 lds\n" \
"buffer_load_dwordx4 v200, s[40:43], s58 offen offset:3072 sc1 lds\n" \
"s_add_u32 m0, s56, 110592\n" \
"s_mov_b32 s58, 110592\n" \
"buffer_load_dwordx4 v200, s[40:43], s58 offen offset:0 sc1 lds\n" \
"buffer_load_dwordx4 v200, s[40:43], s58 offen offset:1024 sc1 lds\n" \
"buffer_load_dwordx4 v200, s[40:43], s58 offen offset:2048 sc1 lds\n" \
"buffer_load_dwordx4 v200, s[40:43], s58 offen offset:3072 sc1 lds\n" \
"s_add_u32 m0, s56, 36864\n" \
"s_mov_b32 s58, 36864\n" \
"buffer_load_dwordx4 v200, s[40:43], s58 offen offset:0 sc1 lds\n" \
"buffer_load_dwordx4 v200, s[40:43], s58 offen offset:1024 sc1 lds\n" \
"buffer_load_dwordx4 v200, s[40:43], s58 offen offset:2048 sc1 lds\n" \
"buffer_load_dwordx4 v200, s[40:43], s58 offen offset:3072 sc1 lds\n" \
"s_add_u32 m0, s56, 114688\n" \
"s_mov_b32 s58, 114688\n" \
"buffer_load_dwordx4 v200, s[40:43], s58 offen offset:0 sc1 lds\n" \
"buffer_load_dwordx4 v200, s[40:43], s58 offen offset:1024 sc1 lds\n" \
"buffer_load_dwordx4 v200, s[40:43], s58 offen offset:2048 sc1 lds\n" \
"buffer_load_dwordx4 v200, s[40:43], s58 offen offset:3072 sc1 lds\n" \
"s_waitcnt vmcnt(54)\n" \
"s_add_u32 m0, s56, 40960\n" \
"s_mov_b32 s58, 40960\n" \
"buffer_load_dwordx4 v200, s[40:43], s58 offen offset:0 sc1 lds\n" \
"buffer_load_dwordx4 v200, s[40:43], s58 offen offset:1024 sc1 lds\n" \
"buffer_load_dwordx4 v200, s[40:43], s58 offen offset:2048 sc1 lds\n" \
"buffer_load_dwordx4 v200, s[40:43], s58 offen offset:3072 sc1 lds\n" \
"s_add_u32 m0, s56, 118784\n" \
"s_mov_b32 s58, 118784\n" \
"buffer_load_dwordx4 v200, s[40:43], s58 offen offset:0 sc1 lds\n" \
"buffer_load_dwordx4 v200, s[40:43], s58 offen offset:1024 sc1 lds\n" \
"buffer_load_dwordx4 v200, s[40:43], s58 offen offset:2048 sc1 lds\n" \
"buffer_load_dwordx4 v200, s[40:43], s58 offen offset:3072 sc1 lds\n" \
"s_waitcnt vmcnt(54)\n" \
"s_waitcnt vmcnt(40)\n" \
"s_barrier\n" \
"s_add_u32 m0, s56, 45056\n" \
"s_mov_b32 s58, 45056\n" \
"buffer_load_dwordx4 v200, s[40:43], s58 offen offset:0 sc1 lds\n" \
"buffer_load_dwordx4 v200, s[40:43], s58 offen offset:1024 sc1 lds\n" \
"buffer_load_dwordx4 v200, s[40:43], s58 offen offset:2048 sc1 lds\n" \
"buffer_load_dwordx4 v200, s[40:43], s58 offen offset:3072 sc1 lds\n" \
"s_add_u32 m0, s56, 122880\n" \
"s_mov_b32 s58, 122880\n" \
"buffer_load_dwordx4 v200, s[40:43], s58 offen offset:0 sc1 lds\n" \
"buffer_load_dwordx4 v200, s[40:43], s58 offen offset:1024 sc1 lds\n" \
"buffer_load_dwordx4 v200, s[40:43], s58 offen offset:2048 sc1 lds\n" \
"buffer_load_dwordx4 v200, s[40:43], s58 offen offset:3072 sc1 lds\n" \
"s_waitcnt vmcnt(54)\n" \
"s_add_u32 m0, s56, 49152\n" \
"s_mov_b32 s58, 49152\n" \
"buffer_load_dwordx4 v200, s[40:43], s58 offen offset:0 sc1 lds\n" \
"buffer_load_dwordx4 v200, s[40:43], s58 offen offset:1024 sc1 lds\n" \
"buffer_load_dwordx4 v200, s[40:43], s58 offen offset:2048 sc1 lds\n" \
"buffer_load_dwordx4 v200, s[40:43], s58 offen offset:3072 sc1 lds\n" \
"s_add_u32 m0, s56, 126976\n" \
"s_mov_b32 s58, 126976\n" \
"buffer_load_dwordx4 v200, s[40:43], s58 offen offset:0 sc1 lds\n" \
"buffer_load_dwordx4 v200, s[40:43], s58 offen offset:1024 sc1 lds\n" \
"buffer_load_dwordx4 v200, s[40:43], s58 offen offset:2048 sc1 lds\n" \
"buffer_load_dwordx4 v200, s[40:43], s58 offen offset:3072 sc1 lds\n" \
"s_waitcnt vmcnt(54)\n" \
"s_add_u32 m0, s56, 53248\n" \
"s_mov_b32 s58, 53248\n" \
"buffer_load_dwordx4 v200, s[40:43], s58 offen offset:0 sc1 lds\n" \
"buffer_load_dwordx4 v200, s[40:43], s58 offen offset:1024 sc1 lds\n" \
"buffer_load_dwordx4 v200, s[40:43], s58 offen offset:2048 sc1 lds\n" \
"buffer_load_dwordx4 v200, s[40:43], s58 offen offset:3072 sc1 lds\n" \
"s_add_u32 m0, s56, 131072\n" \
"s_mov_b32 s58, 131072\n" \
"buffer_load_dwordx4 v200, s[40:43], s58 offen offset:0 sc1 lds\n" \
"buffer_load_dwordx4 v200, s[40:43], s58 offen offset:1024 sc1 lds\n" \
"buffer_load_dwordx4 v200, s[40:43], s58 offen offset:2048 sc1 lds\n" \
"buffer_load_dwordx4 v200, s[40:43], s58 offen offset:3072 sc1 lds\n" \
"s_waitcnt vmcnt(54)\n" \
"s_add_u32 m0, s56, 57344\n" \
"s_mov_b32 s58, 57344\n" \
"buffer_load_dwordx4 v200, s[40:43], s58 offen offset:0 sc1 lds\n" \
"buffer_load_dwordx4 v200, s[40:43], s58 offen offset:1024 sc1 lds\n" \
"buffer_load_dwordx4 v200, s[40:43], s58 offen offset:2048 sc1 lds\n" \
"buffer_load_dwordx4 v200, s[40:43], s58 offen offset:3072 sc1 lds\n" \
"s_add_u32 m0, s56, 135168\n" \
"s_mov_b32 s58, 135168\n" \
"buffer_load_dwordx4 v200, s[40:43], s58 offen offset:0 sc1 lds\n" \
"buffer_load_dwordx4 v200, s[40:43], s58 offen offset:1024 sc1 lds\n" \
"buffer_load_dwordx4 v200, s[40:43], s58 offen offset:2048 sc1 lds\n" \
"buffer_load_dwordx4 v200, s[40:43], s58 offen offset:3072 sc1 lds\n" \
"s_waitcnt vmcnt(54)\n" \
"s_add_u32 m0, s56, 61440\n" \
"s_mov_b32 s58, 61440\n" \
"buffer_load_dwordx4 v200, s[40:43], s58 offen offset:0 sc1 lds\n" \
"buffer_load_dwordx4 v200, s[40:43], s58 offen offset:1024 sc1 lds\n" \
"buffer_load_dwordx4 v200, s[40:43], s58 offen offset:2048 sc1 lds\n" \
"buffer_load_dwordx4 v200, s[40:43], s58 offen offset:3072 sc1 lds\n" \
"s_add_u32 m0, s56, 139264\n" \
"s_mov_b32 s58, 139264\n" \
"buffer_load_dwordx4 v200, s[40:43], s58 offen offset:0 sc1 lds\n" \
"buffer_load_dwordx4 v200, s[40:43], s58 offen offset:1024 sc1 lds\n" \
"buffer_load_dwordx4 v200, s[40:43], s58 offen offset:2048 sc1 lds\n" \
"buffer_load_dwordx4 v200, s[40:43], s58 offen offset:3072 sc1 lds\n" \
"s_waitcnt vmcnt(54)\n" \
"s_add_u32 m0, s56, 65536\n" \
"s_mov_b32 s58, 65536\n" \
"buffer_load_dwordx4 v200, s[40:43], s58 offen offset:0 sc1 lds\n" \
"buffer_load_dwordx4 v200, s[40:43], s58 offen offset:1024 sc1 lds\n" \
"buffer_load_dwordx4 v200, s[40:43], s58 offen offset:2048 sc1 lds\n" \
"buffer_load_dwordx4 v200, s[40:43], s58 offen offset:3072 sc1 lds\n" \
"s_add_u32 m0, s56, 143360\n" \
"s_mov_b32 s58, 143360\n" \
"buffer_load_dwordx4 v200, s[40:43], s58 offen offset:0 sc1 lds\n" \
"buffer_load_dwordx4 v200, s[40:43], s58 offen offset:1024 sc1 lds\n" \
"buffer_load_dwordx4 v200, s[40:43], s58 offen offset:2048 sc1 lds\n" \
"buffer_load_dwordx4 v200, s[40:43], s58 offen offset:3072 sc1 lds\n" \
"s_waitcnt vmcnt(54)\n" \
"s_add_u32 m0, s56, 69632\n" \
"s_mov_b32 s58, 69632\n" \
"buffer_load_dwordx4 v200, s[40:43], s58 offen offset:0 sc1 lds\n" \
"buffer_load_dwordx4 v200, s[40:43], s58 offen offset:1024 sc1 lds\n" \
"buffer_load_dwordx4 v200, s[40:43], s58 offen offset:2048 sc1 lds\n" \
"buffer_load_dwordx4 v200, s[40:43], s58 offen offset:3072 sc1 lds\n" \
"s_add_u32 m0, s56, 147456\n" \
"s_mov_b32 s58, 147456\n" \
"buffer_load_dwordx4 v200, s[40:43], s58 offen offset:0 sc1 lds\n" \
"buffer_load_dwordx4 v200, s[40:43], s58 offen offset:1024 sc1 lds\n" \
"buffer_load_dwordx4 v200, s[40:43], s58 offen offset:2048 sc1 lds\n" \
"buffer_load_dwordx4 v200, s[40:43], s58 offen offset:3072 sc1 lds\n" \
"s_waitcnt vmcnt(54)\n" \
"s_add_u32 m0, s56, 73728\n" \
"s_mov_b32 s58, 73728\n" \
"buffer_load_dwordx4 v200, s[40:43], s58 offen offset:0 sc1 lds\n" \
"buffer_load_dwordx4 v200, s[40:43], s58 offen offset:1024 sc1 lds\n" \
"buffer_load_dwordx4 v200, s[40:43], s58 offen offset:2048 sc1 lds\n" \
"buffer_load_dwordx4 v200, s[40:43], s58 offen offset:3072 sc1 lds\n" \
"s_add_u32 m0, s56, 151552\n" \
"s_mov_b32 s58, 151552\n" \
"buffer_load_dwordx4 v200, s[40:43], s58 offen offset:0 sc1 lds\n" \
"buffer_load_dwordx4 v200, s[40:43], s58 offen offset:1024 sc1 lds\n" \
"buffer_load_dwordx4 v200, s[40:43], s58 offen offset:2048 sc1 lds\n" \
"buffer_load_dwordx4 v200, s[40:43], s58 offen offset:3072 sc1 lds\n" \
"s_waitcnt vmcnt(54)\n" \
"s_waitcnt vmcnt(54)\n" \
"s_barrier\n" \
"s_waitcnt vmcnt(38)\n" \
"s_mov_b32 s58, 0\n" \
"buffer_store_dwordx4 v[204:207], v200, s[44:47], s58 offen offset:0 sc1\n" \
"buffer_store_dwordx4 v[204:207], v200, s[44:47], s58 offen offset:1024 sc1\n" \
"buffer_store_dwordx4 v[204:207], v200, s[44:47], s58 offen offset:2048 sc1\n" \
"buffer_store_dwordx4 v[204:207], v200, s[44:47], s58 offen offset:3072 sc1\n" \
"s_mov_b32 s58, 4096\n" \
"buffer_store_dwordx4 v[204:207], v200, s[44:47], s58 offen offset:0 sc1\n" \
"buffer_store_dwordx4 v[204:207], v200, s[44:47], s58 offen offset:1024 sc1\n" \
"buffer_store_dwordx4 v[204:207], v200, s[44:47], s58 offen offset:2048 sc1\n" \
"buffer_store_dwordx4 v[204:207], v200, s[44:47], s58 offen offset:3072 sc1\n" \
"s_mov_b32 s58, 8192\n" \
"buffer_store_dwordx4 v[204:207], v200, s[44:47], s58 offen offset:0 sc1\n" \
"buffer_store_dwordx4 v[204:207], v200, s[44:47], s58 offen offset:1024 sc1\n" \
"buffer_store_dwordx4 v[204:207], v200, s[44:47], s58 offen offset:2048 sc1\n" \
"buffer_store_dwordx4 v[204:207], v200, s[44:47], s58 offen offset:3072 sc1\n" \
"s_mov_b32 s58, 12288\n" \
"buffer_store_dwordx4 v[204:207], v200, s[44:47], s58 offen offset:0 sc1\n" \
"buffer_store_dwordx4 v[204:207], v200, s[44:47], s58 offen offset:1024 sc1\n" \
"buffer_store_dwordx4 v[204:207], v200, s[44:47], s58 offen offset:2048 sc1\n" \
"buffer_store_dwordx4 v[204:207], v200, s[44:47], s58 offen offset:3072 sc1\n" \
"s_mov_b32 s58, 16384\n" \
"buffer_store_dwordx4 v[204:207], v200, s[44:47], s58 offen offset:0 sc1\n" \
"buffer_store_dwordx4 v[204:207], v200, s[44:47], s58 offen offset:1024 sc1\n" \
"buffer_store_dwordx4 v[204:207], v200, s[44:47], s58 offen offset:2048 sc1\n" \
"buffer_store_dwordx4 v[204:207], v200, s[44:47], s58 offen offset:3072 sc1\n" \
"s_mov_b32 s58, 20480\n" \
"buffer_store_dwordx4 v[204:207], v200, s[44:47], s58 offen offset:0 sc1\n" \
"buffer_store_dwordx4 v[204:207], v200, s[44:47], s58 offen offset:1024 sc1\n" \
"buffer_store_dwordx4 v[204:207], v200, s[44:47], s58 offen offset:2048 sc1\n" \
"buffer_store_dwordx4 v[204:207], v200, s[44:47], s58 offen offset:3072 sc1\n" \
"s_waitcnt vmcnt(24)\n" \
"s_barrier\n"

#define SVC_EARLY \
"s_mov_b32 s58, 0\n" \
"buffer_load_dwordx4 v[0:3], v200, s[40:43], s58 offen offset:0 sc1\n" \
"buffer_load_dwordx4 v[4:7], v200, s[40:43], s58 offen offset:1024 sc1\n" \
"buffer_load_dwordx4 v[8:11], v200, s[40:43], s58 offen offset:2048 sc1\n" \
"buffer_load_dwordx4 v[12:15], v200, s[40:43], s58 offen offset:3072 sc1\n" \
"s_mov_b32 s58, 4096\n" \
"buffer_load_dwordx4 v[16:19], v200, s[40:43], s58 offen offset:0 sc1\n" \
"buffer_load_dwordx4 v[20:23], v200, s[40:43], s58 offen offset:1024 sc1\n" \
"buffer_load_dwordx4 v[24:27], v200, s[40:43], s58 offen offset:2048 sc1\n" \
"buffer_load_dwordx4 v[28:31], v200, s[40:43], s58 offen offset:3072 sc1\n" \
"s_mov_b32 s58, 8192\n" \
"buffer_load_dwordx4 v[32:35], v200, s[40:43], s58 offen offset:0 sc1\n" \
"buffer_load_dwordx4 v[36:39], v200, s[40:43], s58 offen offset:1024 sc1\n" \
"buffer_load_dwordx4 v[40:43], v200, s[40:43], s58 offen offset:2048 sc1\n" \
"buffer_load_dwordx4 v[44:47], v200, s[40:43], s58 offen offset:3072 sc1\n" \
"s_mov_b32 s58, 12288\n" \
"buffer_load_dwordx4 v[48:51], v200, s[40:43], s58 offen offset:0 sc1\n" \
"buffer_load_dwordx4 v[52:55], v200, s[40:43], s58 offen offset:1024 sc1\n" \
"buffer_load_dwordx4 v[56:59], v200, s[40:43], s58 offen offset:2048 sc1\n" \
"buffer_load_dwordx4 v[60:63], v200, s[40:43], s58 offen offset:3072 sc1\n" \
"s_mov_b32 s58, 16384\n" \
"buffer_load_dwordx4 v[64:67], v200, s[40:43], s58 offen offset:0 sc1\n" \
"buffer_load_dwordx4 v[68:71], v200, s[40:43], s58 offen offset:1024 sc1\n" \
"buffer_load_dwordx4 v[72:75], v208, s[44:47], 0 offen\n" \
"buffer_load_dwordx4 v[76:79], v209, s[44:47], 0 offen\n" \
"buffer_load_dwordx4 v[80:83], v210, s[44:47], 0 offen\n" \
"buffer_load_dwordx4 v[84:87], v211, s[44:47], 0 offen\n" \
"buffer_load_dwordx4 v[88:91], v212, s[44:47], 0 offen\n" \
"buffer_load_dwordx4 v[92:95], v213, s[44:47], 0 offen\n" \
"buffer_load_dwordx4 v[96:99], v214, s[44:47], 0 offen\n" \
"buffer_load_dwordx4 v[100:103], v215, s[44:47], 0 offen\n" \
"buffer_load_dwordx4 v[104:107], v216, s[44:47], 0 offen\n" \
"s_add_u32 s58, s60, 0\n" \
"buffer_store_dwordx4 v[204:207], v200, s[48:51], s58 offen offset:0 sc1\n" \
"buffer_store_dwordx4 v[204:207], v200, s[48:51], s58 offen offset:1024 sc1\n" \
"buffer_store_dwordx4 v[204:207], v200, s[48:51], s58 offen offset:2048 sc1\n" \
"buffer_store_dwordx4 v[204:207], v200, s[48:51], s58 offen offset:3072 sc1\n" \
"s_add_u32 s58, s60, 4096\n" \
"buffer_store_dwordx4 v[204:207], v200, s[48:51], s58 offen offset:0 sc1\n" \
"buffer_store_dwordx4 v[204:207], v200, s[48:51], s58 offen offset:1024 sc1\n" \
"buffer_store_dwordx4 v[204:207], v200, s[48:51], s58 offen offset:2048 sc1\n" \
"buffer_store_dwordx4 v[204:207], v200, s[48:51], s58 offen offset:3072 sc1\n" \
"s_add_u32 s58, s60, 8192\n" \
"buffer_store_dwordx4 v[204:207], v200, s[48:51], s58 offen offset:0 sc1\n" \
"buffer_store_dwordx4 v[204:207], v200, s[48:51], s58 offen offset:1024 sc1\n" \
"buffer_store_dwordx4 v[204:207], v200, s[48:51], s58 offen offset:2048 sc1\n" \
"buffer_store_dwordx4 v[204:207], v200, s[48:51], s58 offen offset:3072 sc1\n" \
"s_add_u32 s58, s60, 12288\n" \
"s_waitcnt vmcnt(12)\n" \
"buffer_store_dwordx4 v[204:207], v200, s[48:51], s58 offen offset:0 sc1\n" \
"buffer_store_dwordx4 v[204:207], v200, s[48:51], s58 offen offset:1024 sc1\n" \
"buffer_store_dwordx4 v[204:207], v200, s[48:51], s58 offen offset:2048 sc1\n" \
"buffer_store_dwordx4 v[204:207], v200, s[48:51], s58 offen offset:3072 sc1\n" \
"s_add_u32 s58, s60, 16384\n" \
"buffer_store_dwordx4 v[204:207], v200, s[48:51], s58 offen offset:0 sc1\n" \
"buffer_store_dwordx4 v[204:207], v200, s[48:51], s58 offen offset:1024 sc1\n" \
"s_waitcnt vmcnt(18)\n" \
"buffer_store_dwordx4 v[204:207], v200, s[48:51], s58 offen offset:2048 sc1\n" \
"buffer_store_dwordx4 v[204:207], v200, s[48:51], s58 offen offset:3072 sc1\n" \
"s_add_u32 s58, s60, 20480\n" \
"buffer_store_dwordx4 v[204:207], v200, s[48:51], s58 offen offset:0 sc1\n" \
"buffer_store_dwordx4 v[204:207], v200, s[48:51], s58 offen offset:1024 sc1\n" \
"buffer_store_dwordx4 v[204:207], v200, s[48:51], s58 offen offset:2048 sc1\n" \
"buffer_store_dwordx4 v[204:207], v200, s[48:51], s58 offen offset:3072 sc1\n" \
"s_add_u32 s58, s60, 24576\n" \
"s_waitcnt vmcnt(18)\n" \
"buffer_store_dwordx4 v[204:207], v200, s[48:51], s58 offen offset:0 sc1\n" \
"buffer_store_dwordx4 v[204:207], v200, s[48:51], s58 offen offset:1024 sc1\n" \
"buffer_store_dwordx4 v[204:207], v200, s[48:51], s58 offen offset:2048 sc1\n" \
"buffer_store_dwordx4 v[204:207], v200, s[48:51], s58 offen offset:3072 sc1\n" \
"s_add_u32 s58, s60, 28672\n" \
"buffer_store_dwordx4 v[204:207], v200, s[48:51], s58 offen offset:0 sc1\n" \
"buffer_store_dwordx4 v[204:207], v200, s[48:51], s58 offen offset:1024 sc1\n" \
"s_waitcnt vmcnt(18)\n" \
"buffer_store_dwordx4 v[204:207], v200, s[48:51], s58 offen offset:2048 sc1\n" \
"buffer_store_dwordx4 v[204:207], v200, s[48:51], s58 offen offset:3072 sc1\n" \
"s_add_u32 s58, s60, 32768\n" \
"buffer_store_dwordx4 v[204:207], v200, s[48:51], s58 offen offset:0 sc1\n" \
"buffer_store_dwordx4 v[204:207], v200, s[48:51], s58 offen offset:1024 sc1\n" \
"buffer_store_dwordx4 v[204:207], v200, s[48:51], s58 offen offset:2048 sc1\n" \
"buffer_store_dwordx4 v[204:207], v200, s[48:51], s58 offen offset:3072 sc1\n" \
"s_add_u32 s58, s60, 36864\n" \
"s_waitcnt vmcnt(18)\n" \
"buffer_store_dwordx4 v[204:207], v200, s[48:51], s58 offen offset:0 sc1\n" \
"buffer_store_dwordx4 v[204:207], v200, s[48:51], s58 offen offset:1024 sc1\n" \
"buffer_store_dwordx4 v[204:207], v200, s[48:51], s58 offen offset:2048 sc1\n" \
"s_waitcnt vmcnt(12)\n"

#define E2_FETCH \
"s_bitcmp1_b32 %20, 0\n" \
"s_cbranch_scc0 Lsk0_%=\n" \
"s_add_u32 %16, %21, 0\n" \
"buffer_load_dwordx4 %0, %17, %19, %16 offen sc1\n" \
"buffer_load_dwordx2 %8, %18, %19, %16 offen sc1\n" \
"Lsk0_%=:\n" \
"s_bitcmp1_b32 %20, 1\n" \
"s_cbranch_scc0 Lsk1_%=\n" \
"s_add_u32 %16, %21, 6144\n" \
"buffer_load_dwordx4 %1, %17, %19, %16 offen sc1\n" \
"buffer_load_dwordx2 %9, %18, %19, %16 offen sc1\n" \
"Lsk1_%=:\n" \
"s_bitcmp1_b32 %20, 2\n" \
"s_cbranch_scc0 Lsk2_%=\n" \
"s_add_u32 %16, %21, 12288\n" \
"buffer_load_dwordx4 %2, %17, %19, %16 offen sc1\n" \
"buffer_load_dwordx2 %10, %18, %19, %16 offen sc1\n" \
"Lsk2_%=:\n" \
"s_bitcmp1_b32 %20, 3\n" \
"s_cbranch_scc0 Lsk3_%=\n" \
"s_add_u32 %16, %21, 18432\n" \
"buffer_load_dwordx4 %3, %17, %19, %16 offen sc1\n" \
"buffer_load_dwordx2 %11, %18, %19, %16 offen sc1\n" \
"Lsk3_%=:\n" \
"s_bitcmp1_b32 %20, 4\n" \
"s_cbranch_scc0 Lsk4_%=\n" \
"s_add_u32 %16, %22, 0\n" \
"buffer_load_dwordx4 %4, %17, %19, %16 offen sc1\n" \
"buffer_load_dwordx2 %12, %18, %19, %16 offen sc1\n" \
"Lsk4_%=:\n" \
"s_bitcmp1_b32 %20, 5\n" \
"s_cbranch_scc0 Lsk5_%=\n" \
"s_add_u32 %16, %22, 6144\n" \
"buffer_load_dwordx4 %5, %17, %19, %16 offen sc1\n" \
"buffer_load_dwordx2 %13, %18, %19, %16 offen sc1\n" \
"Lsk5_%=:\n" \
"s_bitcmp1_b32 %20, 6\n" \
"s_cbranch_scc0 Lsk6_%=\n" \
"s_add_u32 %16, %22, 12288\n" \
"buffer_load_dwordx4 %6, %17, %19, %16 offen sc1\n" \
"buffer_load_dwordx2 %14, %18, %19, %16 offen sc1\n" \
"Lsk6_%=:\n" \
"s_bitcmp1_b32 %20, 7\n" \
"s_cbranch_scc0 Lsk7_%=\n" \
"s_add_u32 %16, %22, 18432\n" \
"buffer_load_dwordx4 %7, %17, %19, %16 offen sc1\n" \
"buffer_load_dwordx2 %15, %18, %19, %16 offen sc1\n" \
"Lsk7_%=:\n"
#define E2_FETCH_LATE \
"s_bitcmp1_b32 %23, 0\n" \
"s_cbranch_scc0 Lsl0_%=\n" \
"s_add_u32 %16, %21, 0\n" \
"buffer_load_dwordx4 %0, %17, %19, %16 offen sc1\n" \
"buffer_load_dwordx2 %8, %18, %19, %16 offen sc1\n" \
"Lsl0_%=:\n" \
"s_bitcmp1_b32 %23, 1\n" \
"s_cbranch_scc0 Lsl1_%=\n" \
"s_add_u32 %16, %21, 6144\n" \
"buffer_load_dwordx4 %1, %17, %19, %16 offen sc1\n" \
"buffer_load_dwordx2 %9, %18, %19, %16 offen sc1\n" \
"Lsl1_%=:\n" \
"s_bitcmp1_b32 %23, 2\n" \
"s_cbranch_scc0 Lsl2_%=\n" \
"s_add_u32 %16, %21, 12288\n" \
"buffer_load_dwordx4 %2, %17, %19, %16 offen sc1\n" \
"buffer_load_dwordx2 %10, %18, %19, %16 offen sc1\n" \
"Lsl2_%=:\n" \
"s_bitcmp1_b32 %23, 3\n" \
"s_cbranch_scc0 Lsl3_%=\n" \
"s_add_u32 %16, %21, 18432\n" \
"buffer_load_dwordx4 %3, %17, %19, %16 offen sc1\n" \
"buffer_load_dwordx2 %11, %18, %19, %16 offen sc1\n" \
"Lsl3_%=:\n" \
"s_bitcmp1_b32 %23, 4\n" \
"s_cbranch_scc0 Lsl4_%=\n" \
"s_add_u32 %16, %22, 0\n" \
"buffer_load_dwordx4 %4, %17, %19, %16 offen sc1\n" \
"buffer_load_dwordx2 %12, %18, %19, %16 offen sc1\n" \
"Lsl4_%=:\n" \
"s_bitcmp1_b32 %23, 5\n" \
"s_cbranch_scc0 Lsl5_%=\n" \
"s_add_u32 %16, %22, 6144\n" \
"buffer_load_dwordx4 %5, %17, %19, %16 offen sc1\n" \
"buffer_load_dwordx2 %13, %18, %19, %16 offen sc1\n" \
"Lsl5_%=:\n" \
"s_bitcmp1_b32 %23, 6\n" \
"s_cbranch_scc0 Lsl6_%=\n" \
"s_add_u32 %16, %22, 12288\n" \
"buffer_load_dwordx4 %6, %17, %19, %16 offen sc1\n" \
"buffer_load_dwordx2 %14, %18, %19, %16 offen sc1\n" \
"Lsl6_%=:\n" \
"s_bitcmp1_b32 %23, 7\n" \
"s_cbranch_scc0 Lsl7_%=\n" \
"s_add_u32 %16, %22, 18432\n" \
"buffer_load_dwordx4 %7, %17, %19, %16 offen sc1\n" \
"buffer_load_dwordx2 %15, %18, %19, %16 offen sc1\n" \
"Lsl7_%=:\n"

#define P3_ASM2 \
"s_mov_b32 s60, s56\n" \
"s_add_u32 s61, s56, 73728\n" \
"buffer_load_dwordx4 v[0:3], v192, s[40:43], s60 offen offset:0 sc1\n" \
"buffer_load_dwordx4 v[72:75], v192, s[40:43], s61 offen offset:0 sc1\n" \
"buffer_load_dwordx4 v[4:7], v192, s[40:43], s60 offen offset:1024 sc1\n" \
"buffer_load_dwordx4 v[76:79], v192, s[40:43], s61 offen offset:1024 sc1\n" \
"buffer_load_dwordx4 v[8:11], v192, s[40:43], s60 offen offset:2048 sc1\n" \
"buffer_load_dwordx4 v[80:83], v192, s[40:43], s61 offen offset:2048 sc1\n" \
"buffer_load_dwordx4 v[12:15], v192, s[40:43], s60 offen offset:3072 sc1\n" \
"buffer_load_dwordx4 v[84:87], v192, s[40:43], s61 offen offset:3072 sc1\n" \
"s_add_u32 s60, s60, 4096\n" \
"s_add_u32 s61, s61, 4096\n" \
"buffer_load_dwordx4 v[16:19], v192, s[40:43], s60 offen offset:0 sc1\n" \
"buffer_load_dwordx4 v[88:91], v192, s[40:43], s61 offen offset:0 sc1\n" \
"buffer_load_dwordx4 v[20:23], v192, s[40:43], s60 offen offset:1024 sc1\n" \
"buffer_load_dwordx4 v[92:95], v192, s[40:43], s61 offen offset:1024 sc1\n" \
"buffer_load_dwordx4 v[24:27], v192, s[40:43], s60 offen offset:2048 sc1\n" \
"buffer_load_dwordx4 v[96:99], v192, s[40:43], s61 offen offset:2048 sc1\n" \
"buffer_load_dwordx4 v[28:31], v192, s[40:43], s60 offen offset:3072 sc1\n" \
"buffer_load_dwordx4 v[100:103], v192, s[40:43], s61 offen offset:3072 sc1\n" \
"s_add_u32 s60, s60, 4096\n" \
"s_add_u32 s61, s61, 4096\n" \
"buffer_load_dwordx4 v[32:35], v192, s[40:43], s60 offen offset:0 sc1\n" \
"buffer_load_dwordx4 v[104:107], v192, s[40:43], s61 offen offset:0 sc1\n" \
"buffer_load_dwordx4 v[36:39], v192, s[40:43], s60 offen offset:1024 sc1\n" \
"buffer_load_dwordx4 v[108:111], v192, s[40:43], s61 offen offset:1024 sc1\n" \
"buffer_load_dwordx4 v[40:43], v192, s[40:43], s60 offen offset:2048 sc1\n" \
"buffer_load_dwordx4 v[112:115], v192, s[40:43], s61 offen offset:2048 sc1\n" \
"buffer_load_dwordx4 v[44:47], v192, s[40:43], s60 offen offset:3072 sc1\n" \
"buffer_load_dwordx4 v[116:119], v192, s[40:43], s61 offen offset:3072 sc1\n" \
"s_add_u32 s60, s60, 4096\n" \
"s_add_u32 s61, s61, 4096\n" \
"v_mov_b32 v144, 0\n" \
"v_mov_b32 v145, 0\n" \
"v_mov_b32 v146, 0\n" \
"v_mov_b32 v147, 0\n" \
"v_mov_b32 v148, 0\n" \
"v_mov_b32 v149, 0\n" \
"v_mov_b32 v150, 0\n" \
"v_mov_b32 v151, 0\n" \
"v_mov_b32 v152, 0\n" \
"v_mov_b32 v153, 0\n" \
"v_mov_b32 v154, 0\n" \
"v_mov_b32 v155, 0\n" \
"v_mov_b32 v156, 0\n" \
"v_mov_b32 v157, 0\n" \
"v_mov_b32 v158, 0\n" \
"v_mov_b32 v159, 0\n" \
"v_add_u32 v195, 18432, v194\n" \
"v_add_u32 v196, 1024, v194\n" \
"v_add_u32 v197, 19456, v194\n" \
"s_mov_b32 m0, s59\n" \
"s_barrier\n" \
"buffer_load_dwordx4 v192, s[44:47], s58 offen sc1 lds\n" \
"s_add_u32 m0, m0, 4096\n" \
"s_add_u32 s58, s58, 4096\n" \
"buffer_load_dwordx4 v192, s[44:47], s58 offen sc1 lds\n" \
"s_add_u32 m0, m0, 4096\n" \
"s_add_u32 s58, s58, 4096\n" \
"buffer_load_dwordx4 v192, s[44:47], s58 offen sc1 lds\n" \
"s_add_u32 m0, m0, 4096\n" \
"s_add_u32 s58, s58, 4096\n" \
"buffer_load_dwordx4 v192, s[44:47], s58 offen sc1 lds\n" \
"s_add_u32 m0, m0, 4096\n" \
"s_add_u32 s58, s58, 4096\n" \
"buffer_load_dwordx4 v192, s[44:47], s58 offen sc1 lds\n" \
"s_add_u32 m0, m0, 4096\n" \
"s_add_u32 s58, s58, 4096\n" \
"buffer_load_dwordx4 v192, s[44:47], s58 offen sc1 lds\n" \
"s_add_u32 m0, m0, 4096\n" \
"s_add_u32 s58, s58, 4096\n" \
"buffer_load_dwordx4 v192, s[44:47], s58 offen sc1 lds\n" \
"s_add_u32 m0, m0, 4096\n" \
"s_add_u32 s58, s58, 4096\n" \
"buffer_load_dwordx4 v192, s[44:47], s58 offen sc1 lds\n" \
"s_add_u32 m0, m0, 4096\n" \
"s_add_u32 s58, s58, 4096\n" \
"buffer_load_dwordx4 v192, s[44:47], s58 offen sc1 lds\n" \
"s_waitcnt vmcnt(0)\n" \
"s_barrier\n" \
"ds_read_b128 v[160:163], v193 offset:0\n" \
"ds_read_b128 v[164:167], v193 offset:18432\n" \
"ds_read_b128 v[168:171], v193 offset:1024\n" \
"ds_read_b128 v[172:175], v193 offset:19456\n" \
"ds_read_b128 v[176:179], v193 offset:2048\n" \
"ds_read_b128 v[180:183], v193 offset:20480\n" \
"ds_read_b128 v[184:187], v193 offset:3072\n" \
"ds_read_b128 v[188:191], v193 offset:21504\n" \
"s_waitcnt lgkmcnt(6)\n" \
"v_mfma_f32_16x16x32_bf16 v[144:147], v[0:3], v[160:163], v[144:147]\n" \
"v_mfma_f32_16x16x32_bf16 v[148:151], v[0:3], v[164:167], v[148:151]\n" \
"v_mfma_f32_16x16x32_bf16 v[152:155], v[72:75], v[160:163], v[152:155]\n" \
"v_mfma_f32_16x16x32_bf16 v[156:159], v[72:75], v[164:167], v[156:159]\n" \
"buffer_load_dwordx4 v[48:51], v192, s[40:43], s60 offen offset:0 sc1\n" \
"buffer_load_dwordx4 v[120:123], v192, s[40:43], s61 offen offset:0 sc1\n" \
"ds_read_b128 v[160:163], v193 offset:4096\n" \
"ds_read_b128 v[164:167], v193 offset:22528\n" \
"s_waitcnt lgkmcnt(6)\n" \
"v_mfma_f32_16x16x32_bf16 v[144:147], v[4:7], v[168:171], v[144:147]\n" \
"v_mfma_f32_16x16x32_bf16 v[148:151], v[4:7], v[172:175], v[148:151]\n" \
"v_mfma_f32_16x16x32_bf16 v[152:155], v[76:79], v[168:171], v[152:155]\n" \
"v_mfma_f32_16x16x32_bf16 v[156:159], v[76:79], v[172:175], v[156:159]\n" \
"buffer_load_dwordx4 v[52:55], v192, s[40:43], s60 offen offset:1024 sc1\n" \
"buffer_load_dwordx4 v[124:127], v192, s[40:43], s61 offen offset:1024 sc1\n" \
"ds_read_b128 v[168:171], v193 offset:5120\n" \
"ds_read_b128 v[172:175], v193 offset:23552\n" \
"s_waitcnt lgkmcnt(6)\n" \
"v_mfma_f32_16x16x32_bf16 v[144:147], v[8:11], v[176:179], v[144:147]\n" \
"v_mfma_f32_16x16x32_bf16 v[148:151], v[8:11], v[180:183], v[148:151]\n" \
"v_mfma_f32_16x16x32_bf16 v[152:155], v[80:83], v[176:179], v[152:155]\n" \
"v_mfma_f32_16x16x32_bf16 v[156:159], v[80:83], v[180:183], v[156:159]\n" \
"buffer_load_dwordx4 v[56:59], v192, s[40:43], s60 offen offset:2048 sc1\n" \
"buffer_load_dwordx4 v[128:131], v192, s[40:43], s61 offen offset:2048 sc1\n" \
"ds_read_b128 v[176:179], v193 offset:6144\n" \
"ds_read_b128 v[180:183], v193 offset:24576\n" \
"s_waitcnt lgkmcnt(6)\n" \
"v_mfma_f32_16x16x32_bf16 v[144:147], v[12:15], v[184:187], v[144:147]\n" \
"v_mfma_f32_16x16x32_bf16 v[148:151], v[12:15], v[188:191], v[148:151]\n" \
"v_mfma_f32_16x16x32_bf16 v[152:155], v[84:87], v[184:187], v[152:155]\n" \
"v_mfma_f32_16x16x32_bf16 v[156:159], v[84:87], v[188:191], v[156:159]\n" \
"buffer_load_dwordx4 v[60:63], v192, s[40:43], s60 offen offset:3072 sc1\n" \
"buffer_load_dwordx4 v[132:135], v192, s[40:43], s61 offen offset:3072 sc1\n" \
"s_add_u32 s60, s60, 4096\n" \
"s_add_u32 s61, s61, 4096\n" \
"ds_read_b128 v[184:187], v193 offset:7168\n" \
"ds_read_b128 v[188:191], v193 offset:25600\n" \
"s_waitcnt lgkmcnt(6)\n" \
"v_mfma_f32_16x16x32_bf16 v[144:147], v[16:19], v[160:163], v[144:147]\n" \
"v_mfma_f32_16x16x32_bf16 v[148:151], v[16:19], v[164:167], v[148:151]\n" \
"v_mfma_f32_16x16x32_bf16 v[152:155], v[88:91], v[160:163], v[152:155]\n" \
"v_mfma_f32_16x16x32_bf16 v[156:159], v[88:91], v[164:167], v[156:159]\n" \
"buffer_load_dwordx4 v[64:67], v192, s[40:43], s60 offen offset:0 sc1\n" \
"buffer_load_dwordx4 v[136:139], v192, s[40:43], s61 offen offset:0 sc1\n" \
"ds_read_b128 v[160:163], v193 offset:8192\n" \
"ds_read_b128 v[164:167], v193 offset:26624\n" \
"s_waitcnt lgkmcnt(6)\n" \
"v_mfma_f32_16x16x32_bf16 v[144:147], v[20:23], v[168:171], v[144:147]\n" \
"v_mfma_f32_16x16x32_bf16 v[148:151], v[20:23], v[172:175], v[148:151]\n" \
"v_mfma_f32_16x16x32_bf16 v[152:155], v[92:95], v[168:171], v[152:155]\n" \
"v_mfma_f32_16x16x32_bf16 v[156:159], v[92:95], v[172:175], v[156:159]\n" \
"buffer_load_dwordx4 v[68:71], v192, s[40:43], s60 offen offset:1024 sc1\n" \
"buffer_load_dwordx4 v[140:143], v192, s[40:43], s61 offen offset:1024 sc1\n" \
"ds_read_b128 v[168:171], v193 offset:9216\n" \
"ds_read_b128 v[172:175], v193 offset:27648\n" \
"s_waitcnt lgkmcnt(6)\n" \
"v_mfma_f32_16x16x32_bf16 v[144:147], v[24:27], v[176:179], v[144:147]\n" \
"v_mfma_f32_16x16x32_bf16 v[148:151], v[24:27], v[180:183], v[148:151]\n" \
"v_mfma_f32_16x16x32_bf16 v[152:155], v[96:99], v[176:179], v[152:155]\n" \
"v_mfma_f32_16x16x32_bf16 v[156:159], v[96:99], v[180:183], v[156:159]\n" \
"ds_read_b128 v[176:179], v193 offset:10240\n" \
"ds_read_b128 v[180:183], v193 offset:28672\n" \
"s_waitcnt lgkmcnt(6)\n" \
"v_mfma_f32_16x16x32_bf16 v[144:147], v[28:31], v[184:187], v[144:147]\n" \
"v_mfma_f32_16x16x32_bf16 v[148:151], v[28:31], v[188:191], v[148:151]\n" \
"v_mfma_f32_16x16x32_bf16 v[152:155], v[100:103], v[184:187], v[152:155]\n" \
"v_mfma_f32_16x16x32_bf16 v[156:159], v[100:103], v[188:191], v[156:159]\n" \
"ds_read_b128 v[184:187], v193 offset:11264\n" \
"ds_read_b128 v[188:191], v193 offset:29696\n" \
"s_waitcnt lgkmcnt(6)\n" \
"v_mfma_f32_16x16x32_bf16 v[144:147], v[32:35], v[160:163], v[144:147]\n" \
"v_mfma_f32_16x16x32_bf16 v[148:151], v[32:35], v[164:167], v[148:151]\n" \
"v_mfma_f32_16x16x32_bf16 v[152:155], v[104:107], v[160:163], v[152:155]\n" \
"v_mfma_f32_16x16x32_bf16 v[156:159], v[104:107], v[164:167], v[156:159]\n" \
"ds_read_b128 v[160:163], v193 offset:12288\n" \
"ds_read_b128 v[164:167], v193 offset:30720\n" \
"s_waitcnt lgkmcnt(6)\n" \
"v_mfma_f32_16x16x32_bf16 v[144:147], v[36:39], v[168:171], v[144:147]\n" \
"v_mfma_f32_16x16x32_bf16 v[148:151], v[36:39], v[172:175], v[148:151]\n" \
"v_mfma_f32_16x16x32_bf16 v[152:155], v[108:111], v[168:171], v[152:155]\n" \
"v_mfma_f32_16x16x32_bf16 v[156:159], v[108:111], v[172:175], v[156:159]\n" \
"ds_read_b128 v[168:171], v193 offset:13312\n" \
"ds_read_b128 v[172:175], v193 offset:31744\n" \
"s_waitcnt lgkmcnt(6)\n" \
"v_mfma_f32_16x16x32_bf16 v[144:147], v[40:43], v[176:179], v[144:147]\n" \
"v_mfma_f32_16x16x32_bf16 v[148:151], v[40:43], v[180:183], v[148:151]\n" \
"v_mfma_f32_16x16x32_bf16 v[152:155], v[112:115], v[176:179], v[152:155]\n" \
"v_mfma_f32_16x16x32_bf16 v[156:159], v[112:115], v[180:183], v[156:159]\n" \
"ds_read_b128 v[176:179], v193 offset:14336\n" \
"ds_read_b128 v[180:183], v193 offset:32768\n" \
"s_waitcnt lgkmcnt(6)\n" \
"v_mfma_f32_16x16x32_bf16 v[144:147], v[44:47], v[184:187], v[144:147]\n" \
"v_mfma_f32_16x16x32_bf16 v[148:151], v[44:47], v[188:191], v[148:151]\n" \
"v_mfma_f32_16x16x32_bf16 v[152:155], v[116:119], v[184:187], v[152:155]\n" \
"v_mfma_f32_16x16x32_bf16 v[156:159], v[116:119], v[188:191], v[156:159]\n" \
"ds_read_b128 v[184:187], v193 offset:15360\n" \
"ds_read_b128 v[188:191], v193 offset:33792\n" \
"s_waitcnt lgkmcnt(6) vmcnt(8)\n" \
"v_mfma_f32_16x16x32_bf16 v[144:147], v[48:51], v[160:163], v[144:147]\n" \
"v_mfma_f32_16x16x32_bf16 v[148:151], v[48:51], v[164:167], v[148:151]\n" \
"v_mfma_f32_16x16x32_bf16 v[152:155], v[120:123], v[160:163], v[152:155]\n" \
"v_mfma_f32_16x16x32_bf16 v[156:159], v[120:123], v[164:167], v[156:159]\n" \
"ds_read2_b32 v[160:161], v194 offset0:0 offset1:16\n" \
"ds_read2_b32 v[162:163], v194 offset0:32 offset1:48\n" \
"ds_read2_b32 v[164:165], v195 offset0:0 offset1:16\n" \
"ds_read2_b32 v[166:167], v195 offset0:32 offset1:48\n" \
"s_waitcnt lgkmcnt(8)\n" \
"v_mfma_f32_16x16x32_bf16 v[144:147], v[52:55], v[168:171], v[144:147]\n" \
"v_mfma_f32_16x16x32_bf16 v[148:151], v[52:55], v[172:175], v[148:151]\n" \
"v_mfma_f32_16x16x32_bf16 v[152:155], v[124:127], v[168:171], v[152:155]\n" \
"v_mfma_f32_16x16x32_bf16 v[156:159], v[124:127], v[172:175], v[156:159]\n" \
"ds_read2_b32 v[168:169], v196 offset0:0 offset1:16\n" \
"ds_read2_b32 v[170:171], v196 offset0:32 offset1:48\n" \
"ds_read2_b32 v[172:173], v197 offset0:0 offset1:16\n" \
"ds_read2_b32 v[174:175], v197 offset0:32 offset1:48\n" \
"s_waitcnt lgkmcnt(10) vmcnt(4)\n" \
"v_mfma_f32_16x16x32_bf16 v[144:147], v[56:59], v[176:179], v[144:147]\n" \
"v_mfma_f32_16x16x32_bf16 v[148:151], v[56:59], v[180:183], v[148:151]\n" \
"v_mfma_f32_16x16x32_bf16 v[152:155], v[128:131], v[176:179], v[152:155]\n" \
"v_mfma_f32_16x16x32_bf16 v[156:159], v[128:131], v[180:183], v[156:159]\n" \
"s_waitcnt lgkmcnt(8)\n" \
"v_mfma_f32_16x16x32_bf16 v[144:147], v[60:63], v[184:187], v[144:147]\n" \
"v_mfma_f32_16x16x32_bf16 v[148:151], v[60:63], v[188:191], v[148:151]\n" \
"v_mfma_f32_16x16x32_bf16 v[152:155], v[132:135], v[184:187], v[152:155]\n" \
"v_mfma_f32_16x16x32_bf16 v[156:159], v[132:135], v[188:191], v[156:159]\n" \
"s_waitcnt lgkmcnt(4) vmcnt(0)\n" \
"v_mfma_f32_16x16x32_bf16 v[144:147], v[64:67], v[160:163], v[144:147]\n" \
"v_mfma_f32_16x16x32_bf16 v[148:151], v[64:67], v[164:167], v[148:151]\n" \
"v_mfma_f32_16x16x32_bf16 v[152:155], v[136:139], v[160:163], v[152:155]\n" \
"v_mfma_f32_16x16x32_bf16 v[156:159], v[136:139], v[164:167], v[156:159]\n" \
"s_waitcnt lgkmcnt(0)\n" \
"v_mfma_f32_16x16x32_bf16 v[144:147], v[68:71], v[168:171], v[144:147]\n" \
"v_mfma_f32_16x16x32_bf16 v[148:151], v[68:71], v[172:175], v[148:151]\n" \
"v_mfma_f32_16x16x32_bf16 v[152:155], v[140:143], v[168:171], v[152:155]\n" \
"v_mfma_f32_16x16x32_bf16 v[156:159], v[140:143], v[172:175], v[156:159]\n" \
"s_nop 7\n"
#define P3_ASM1 \
"v_mov_b32 v144, 0\n" \
"v_mov_b32 v145, 0\n" \
"v_mov_b32 v146, 0\n" \
"v_mov_b32 v147, 0\n" \
"v_mov_b32 v148, 0\n" \
"v_mov_b32 v149, 0\n" \
"v_mov_b32 v150, 0\n" \
"v_mov_b32 v151, 0\n" \
"v_add_u32 v195, 18432, v194\n" \
"v_add_u32 v196, 1024, v194\n" \
"v_add_u32 v197, 19456, v194\n" \
"s_barrier\n" \
"ds_read_b128 v[160:163], v193 offset:0\n" \
"ds_read_b128 v[164:167], v193 offset:18432\n" \
"ds_read_b128 v[168:171], v193 offset:1024\n" \
"ds_read_b128 v[172:175], v193 offset:19456\n" \
"ds_read_b128 v[176:179], v193 offset:2048\n" \
"ds_read_b128 v[180:183], v193 offset:20480\n" \
"ds_read_b128 v[184:187], v193 offset:3072\n" \
"ds_read_b128 v[188:191], v193 offset:21504\n" \
"s_waitcnt lgkmcnt(6)\n" \
"v_mfma_f32_16x16x32_bf16 v[144:147], v[0:3], v[160:163], v[144:147]\n" \
"v_mfma_f32_16x16x32_bf16 v[148:151], v[0:3], v[164:167], v[148:151]\n" \
"ds_read_b128 v[160:163], v193 offset:4096\n" \
"ds_read_b128 v[164:167], v193 offset:22528\n" \
"s_waitcnt lgkmcnt(6)\n" \
"v_mfma_f32_16x16x32_bf16 v[144:147], v[4:7], v[168:171], v[144:147]\n" \
"v_mfma_f32_16x16x32_bf16 v[148:151], v[4:7], v[172:175], v[148:151]\n" \
"ds_read_b128 v[168:171], v193 offset:5120\n" \
"ds_read_b128 v[172:175], v193 offset:23552\n" \
"s_waitcnt lgkmcnt(6)\n" \
"v_mfma_f32_16x16x32_bf16 v[144:147], v[8:11], v[176:179], v[144:147]\n" \
"v_mfma_f32_16x16x32_bf16 v[148:151], v[8:11], v[180:183], v[148:151]\n" \
"ds_read_b128 v[176:179], v193 offset:6144\n" \
"ds_read_b128 v[180:183], v193 offset:24576\n" \
"s_waitcnt lgkmcnt(6)\n" \
"v_mfma_f32_16x16x32_bf16 v[144:147], v[12:15], v[184:187], v[144:147]\n" \
"v_mfma_f32_16x16x32_bf16 v[148:151], v[12:15], v[188:191], v[148:151]\n" \
"ds_read_b128 v[184:187], v193 offset:7168\n" \
"ds_read_b128 v[188:191], v193 offset:25600\n" \
"s_waitcnt lgkmcnt(6)\n" \
"v_mfma_f32_16x16x32_bf16 v[144:147], v[16:19], v[160:163], v[144:147]\n" \
"v_mfma_f32_16x16x32_bf16 v[148:151], v[16:19], v[164:167], v[148:151]\n" \
"ds_read_b128 v[160:163], v193 offset:8192\n" \
"ds_read_b128 v[164:167], v193 offset:26624\n" \
"s_waitcnt lgkmcnt(6)\n" \
"v_mfma_f32_16x16x32_bf16 v[144:147], v[20:23], v[168:171], v[144:147]\n" \
"v_mfma_f32_16x16x32_bf16 v[148:151], v[20:23], v[172:175], v[148:151]\n" \
"ds_read_b128 v[168:171], v193 offset:9216\n" \
"ds_read_b128 v[172:175], v193 offset:27648\n" \
"s_waitcnt lgkmcnt(6)\n" \
"v_mfma_f32_16x16x32_bf16 v[144:147], v[24:27], v[176:179], v[144:147]\n" \
"v_mfma_f32_16x16x32_bf16 v[148:151], v[24:27], v[180:183], v[148:151]\n" \
"ds_read_b128 v[176:179], v193 offset:10240\n" \
"ds_read_b128 v[180:183], v193 offset:28672\n" \
"s_waitcnt lgkmcnt(6)\n" \
"v_mfma_f32_16x16x32_bf16 v[144:147], v[28:31], v[184:187], v[144:147]\n" \
"v_mfma_f32_16x16x32_bf16 v[148:151], v[28:31], v[188:191], v[148:151]\n" \
"ds_read_b128 v[184:187], v193 offset:11264\n" \
"ds_read_b128 v[188:191], v193 offset:29696\n" \
"s_waitcnt lgkmcnt(6)\n" \
"v_mfma_f32_16x16x32_bf16 v[144:147], v[32:35], v[160:163], v[144:147]\n" \
"v_mfma_f32_16x16x32_bf16 v[148:151], v[32:35], v[164:167], v[148:151]\n" \
"ds_read_b128 v[160:163], v193 offset:12288\n" \
"ds_read_b128 v[164:167], v193 offset:30720\n" \
"s_waitcnt lgkmcnt(6)\n" \
"v_mfma_f32_16x16x32_bf16 v[144:147], v[36:39], v[168:171], v[144:147]\n" \
"v_mfma_f32_16x16x32_bf16 v[148:151], v[36:39], v[172:175], v[148:151]\n" \
"ds_read_b128 v[168:171], v193 offset:13312\n" \
"ds_read_b128 v[172:175], v193 offset:31744\n" \
"s_waitcnt lgkmcnt(6)\n" \
"v_mfma_f32_16x16x32_bf16 v[144:147], v[40:43], v[176:179], v[144:147]\n" \
"v_mfma_f32_16x16x32_bf16 v[148:151], v[40:43], v[180:183], v[148:151]\n" \
"ds_read_b128 v[176:179], v193 offset:14336\n" \
"ds_read_b128 v[180:183], v193 offset:32768\n" \
"s_waitcnt lgkmcnt(6)\n" \
"v_mfma_f32_16x16x32_bf16 v[144:147], v[44:47], v[184:187], v[144:147]\n" \
"v_mfma_f32_16x16x32_bf16 v[148:151], v[44:47], v[188:191], v[148:151]\n" \
"ds_read_b128 v[184:187], v193 offset:15360\n" \
"ds_read_b128 v[188:191], v193 offset:33792\n" \
"s_waitcnt lgkmcnt(6)\n" \
"v_mfma_f32_16x16x32_bf16 v[144:147], v[48:51], v[160:163], v[144:147]\n" \
"v_mfma_f32_16x16x32_bf16 v[148:151], v[48:51], v[164:167], v[148:151]\n" \
"ds_read2_b32 v[160:161], v194 offset0:0 offset1:16\n" \
"ds_read2_b32 v[162:163], v194 offset0:32 offset1:48\n" \
"ds_read2_b32 v[164:165], v195 offset0:0 offset1:16\n" \
"ds_read2_b32 v[166:167], v195 offset0:32 offset1:48\n" \
"s_waitcnt lgkmcnt(8)\n" \
"v_mfma_f32_16x16x32_bf16 v[144:147], v[52:55], v[168:171], v[144:147]\n" \
"v_mfma_f32_16x16x32_bf16 v[148:151], v[52:55], v[172:175], v[148:151]\n" \
"ds_read2_b32 v[168:169], v196 offset0:0 offset1:16\n" \
"ds_read2_b32 v[170:171], v196 offset0:32 offset1:48\n" \
"ds_read2_b32 v[172:173], v197 offset0:0 offset1:16\n" \
"ds_read2_b32 v[174:175], v197 offset0:32 offset1:48\n" \
"s_waitcnt lgkmcnt(10)\n" \
"v_mfma_f32_16x16x32_bf16 v[144:147], v[56:59], v[176:179], v[144:147]\n" \
"v_mfma_f32_16x16x32_bf16 v[148:151], v[56:59], v[180:183], v[148:151]\n" \
"s_waitcnt lgkmcnt(8)\n" \
"v_mfma_f32_16x16x32_bf16 v[144:147], v[60:63], v[184:187], v[144:147]\n" \
"v_mfma_f32_16x16x32_bf16 v[148:151], v[60:63], v[188:191], v[148:151]\n" \
"s_waitcnt lgkmcnt(4)\n" \
"v_mfma_f32_16x16x32_bf16 v[144:147], v[64:67], v[160:163], v[144:147]\n" \
"v_mfma_f32_16x16x32_bf16 v[148:151], v[64:67], v[164:167], v[148:151]\n" \
"s_waitcnt lgkmcnt(0)\n" \
"v_mfma_f32_16x16x32_bf16 v[144:147], v[68:71], v[168:171], v[144:147]\n" \
"v_mfma_f32_16x16x32_bf16 v[148:151], v[68:71], v[172:175], v[148:151]\n" \
"s_nop 7\n"
#define P3_CLOB2 "v0","v1","v2","v3","v4","v5","v6","v7","v8","v9","v10","v11","v12","v13","v14","v15","v16","v17","v18","v19","v20","v21","v22","v23","v24","v25","v26","v27","v28","v29","v30","v31","v32","v33","v34","v35","v36","v37","v38","v39","v40","v41","v42","v43","v44","v45","v46","v47","v48","v49","v50","v51","v52","v53","v54","v55","v56","v57","v58","v59","v60","v61","v62","v63","v64","v65","v66","v67","v68","v69","v70","v71","v72","v73","v74","v75","v76","v77","v78","v79","v80","v81","v82","v83","v84","v85","v86","v87","v88","v89","v90","v91","v92","v93","v94","v95","v96","v97","v98","v99","v100","v101","v102","v103","v104","v105","v106","v107","v108","v109","v110","v111","v112","v113","v114","v115","v116","v117","v118","v119","v120","v121","v122","v123","v124","v125","v126","v127","v128","v129","v130","v131","v132","v133","v134","v135","v136","v137","v138","v139","v140","v141","v142","v143","v160","v161","v162","v163","v164","v165","v166","v167","v168","v169","v170","v171","v172","v173","v174","v175","v176","v177","v178","v179","v180","v181","v182","v183","v184","v185","v186","v187","v188","v189","v190","v191","v195","v196","v197","s60","s61","scc","memory"
#define P3_CLOB1 "v160","v161","v162","v163","v164","v165","v166","v167","v168","v169","v170","v171","v172","v173","v174","v175","v176","v177","v178","v179","v180","v181","v182","v183","v184","v185","v186","v187","v188","v189","v190","v191","v195","v196","v197","memory"


#define MF32(a, b, c) __builtin_amdgcn_mfma_f32_32x32x16_bf16((a), (b), (c), 0, 0, 0)

DI int fresh_lane() { int l; asm volatile("v_mbcnt_lo_u32_b32 %0, -1, 0\n\tv_mbcnt_hi_u32_b32 %0, -1, %0" : "=v"(l)); __builtin_assume(l >= 0 && l < 64); return l; }
DI unsigned rfl(unsigned x) { return __builtin_amdgcn_readfirstlane(x); }
DI unsigned pk2(float lo, float hi) { f32x2 v = {lo, hi}; bf2_t b = __builtin_convertvector(v, bf2_t); return __builtin_bit_cast(unsigned, b); }
DI float bflo(unsigned u) { return __uint_as_float(u << 16); }
DI float bfhi(unsigned u) { return __uint_as_float(u & 0xFFFF0000u); }
DI unsigned nosent(unsigned u) { return min(u, SENT - 1); }
DI u32x4 srd(const void* p, unsigned bytes) { const size_t a = (size_t)p; return u32x4{(unsigned)a, (unsigned)(a >> 32) & 0xFFFFu, bytes, 0x20000u}; }
template <int N> DI void wait_bar() { asm volatile("s_waitcnt vmcnt(%0) lgkmcnt(0)\n\ts_barrier" ::"i"(N) : "memory"); }
DI void lbar() { asm volatile("s_waitcnt lgkmcnt(0)\n\ts_barrier" ::: "memory"); }
DI s16x4 ldstr(lptr p) { return __builtin_amdgcn_ds_read_tr16_b64_v4i16((LAS s16x4*)p); }
typedef __amdgpu_buffer_rsrc_t rsrc_t;
DI rsrc_t rsrc(const void* p, unsigned bytes) { return __builtin_amdgcn_make_buffer_rsrc((void*)p, 0, bytes, 0x20000); }
DI void st128(rsrc_t r, unsigned voff, unsigned soff, u32x4 v) { __builtin_amdgcn_raw_buffer_store_b128(v, r, voff + soff, 0, 16); }
DI void st64(rsrc_t r, unsigned voff, unsigned soff, u32x2 v) { __builtin_amdgcn_raw_buffer_store_b64(v, r, voff, soff, 16); }
DI void st32(rsrc_t r, unsigned voff, unsigned soff, unsigned v) { __builtin_amdgcn_raw_buffer_store_b32(v, r, voff, soff, 16); }
DI u32x4 ld128(rsrc_t r, unsigned voff, unsigned soff) { return __builtin_amdgcn_raw_buffer_load_b128(r, voff, soff, 16); }
DI u32x2 ld64(rsrc_t r, unsigned voff, unsigned soff) { return __builtin_amdgcn_raw_buffer_load_b64(r, voff, soff, 16); }
DI unsigned ld32(rsrc_t r, unsigned voff, unsigned soff) { return __builtin_amdgcn_raw_buffer_load_b32(r, voff, soff, 16); }
DI void fence() { asm volatile("" ::: "memory"); }
DI u32x4 pk8(const float* v) { return u32x4{pk2(v[0], v[1]), pk2(v[2], v[3]), pk2(v[4], v[5]), pk2(v[6], v[7])}; }

DI void publish(char* ws, int stage, u64 tag, bool svc) {
    wait_bar<0>();
    if (svc) st64(rsrc(ws + WS_FL, 5 * 256 * 128), fresh_lane() ? OOB : 0, (stage * 256 + blockIdx.x) * 128, u32x2{(unsigned)tag, (unsigned)(tag >> 32)});
    fence();
}
template <int FIRST = 0>
DI void poll(char* ws, int stage, int wg, u64 tag) {
    if (FIRST) __builtin_amdgcn_s_sleep(FIRST);
    const rsrc_t rf = rsrc(ws + WS_FL + stage * 256 * 128, 256 * 128); const unsigned vo = wg < 0 ? OOB : wg * 128;
#pragma unroll 1
    for (int sp = 0; sp < SPIN_CAP; ++sp) {
        fence(); const u32x2 r = ld64(rf, vo, 0);
        if (__all(wg < 0 || (r[0] == (unsigned)tag && r[1] == (unsigned)(tag >> 32)))) break;
        __builtin_amdgcn_s_sleep(DP);
    }
    fence();
}

template <bool VOR>
DI void p1_body(const char* x, const char* w, char* ws, lptr lds, int rt, int cg, int lane, int wv) {
    const int n = lane & 15, g = lane >> 4, q = n >> 2, p = lane & 3;
    const unsigned l0 = (unsigned)(size_t)lds;
    u32x4 xo, wo4; unsigned wo5; u32x8 wa; unsigned wa8; u32x2 xa;
#pragma unroll
    for (int i = 0; i < 4; ++i) { const int row = 8 * i + (lane >> 3); xo[i] = (unsigned)(row * 1152 + (((lane & 7) ^ ((row >> 1) & 7)) << 4) + 3072 - 1024 * i); }
#pragma unroll
    for (int i = 0; i < 5; ++i) { const int idx = i * 64 + (i < 4 ? lane : lane & 31), pos = (idx * 3641) >> 16, ch = idx - pos * 18;
        const int src = (pos & 3) | ((pos & 4) << 1) | ((pos & 8) >> 1); const unsigned o = (unsigned)(src * 3456 + ch * 16); if (i < 4) wo4[i] = o + 3072 - 1024 * i; else wo5 = o; }
    const u32x4 sx = srd(x + (size_t)(rt * 128 + 32 * wv) * 1152 - 3072, 32 * 1152 + 3072), sw = srd(w + (size_t)16 * wv * 3456 + cg * 288 - 3072, (576 - 16 * wv) * 3456 - cg * 288 + 3072);
    const int rowoff = l0 + (16 * (g >> 1) + 4 * (g & 1) + q) * 288 + 16384;
#pragma unroll
    for (int lt = 0; lt < 8; ++lt) { const int pp = lt >> 1;
        wa[lt] = rowoff + (VOR ? 72 * pp + 40 * (lt & 1) + 8 * p : 72 * pp + 8 * (lt & 1) + (p & 1) * 16 + (p >> 1) * 40); }
    wa8 = rowoff + 72 * p + 32;
    xa[0] = l0 + wv * 4096 + n * 128 + ((g ^ (n >> 1)) << 4); xa[1] = xa[0] ^ 64;
    f32x32 r0, r1; f32x8 r2;
#define P1_RUN(TXT) asm volatile(TXT : "={v[0:31]}"(r0), "={v[32:63]}"(r1), "={v[64:71]}"(r2), "+{v[124:131]}"(wa), "+{v132}"(wa8), "+{v[134:135]}"(xa) \
                 : "{s[40:43]}"(sx), "{s[44:47]}"(sw), "{v[136:139]}"(xo), "{v[140:143]}"(wo4), "{v144}"(wo5), "{s56}"(l0 + wv * 4096), "{s57}"(l0 + 16384 + wv * 4608) : P1_CLOB)
    if (VOR) P1_RUN(P1_ASM_V); else P1_RUN(P1_ASM_QK);
    f32x4 acc[9][2];
#pragma unroll
    for (int k = 0; k < 18; ++k) { const int o = (k & 7) * 4; acc[k >> 1][k & 1] = k < 8 ? f32x4{r0[o], r0[o + 1], r0[o + 2], r0[o + 3]} : k < 16 ? f32x4{r1[o], r1[o + 1], r1[o + 2], r1[o + 3]} : f32x4{r2[o], r2[o + 1], r2[o + 2], r2[o + 3]}; }
    const LAS float* cwt = (const LAS float*)(lds + K1_TAB); const LAS f32x2* stt = (const LAS f32x2*)(lds + K1_TAB + 1024) + wv * 32;
    asm volatile("s_barrier" ::: "memory");
    const int sec = cg >> 2, hb = (cg & 3) * 8, tile32 = rt * 4 + wv;
    if (VOR) {
        const rsrc_t img = rsrc(ws + WS_KV + (size_t)hb * KHS + tile32 * KVT, 7 * KHS + KVT);
        f32x2 st[8]; float c1[9];
#pragma unroll
        for (int i = 0; i < 8; ++i) st[i] = stt[16 * (i >> 2) + 4 * g + (i & 3)];
#pragma unroll
        for (int lt = 0; lt < 9; ++lt) c1[lt] = cwt[lt < 8 ? 36 * (lt >> 1) + 20 * (lt & 1) + n : 36 * q + 16 + p];
#pragma unroll
        for (int lt = 0; lt < 9; ++lt) { float v[8];
#pragma unroll
            for (int i = 0; i < 8; ++i) v[i] = st[i][1] * (acc[lt][i >> 2][i & 3] - st[i][0] * c1[lt]);
            const u32x4 w = pk8(v);
            if (lt < 8) st128(img, 1152 + g * 320 + n * 16, lt * KHS, w);
            else { const unsigned f = p & 1 ? 0u : 0x3F803F80u; const u32x4 fix = {f, f, f, f}; const unsigned vo = 1152 + g * 320 + 256 + q * (2 * KHS);
                st128(img, vo + p * 16, 0, p < 2 ? w : fix); st128(img, vo + (p ^ 2) * 16, KHS, p < 2 ? fix : w); } }
    } else {
        const int tb = sec == 0 ? QT : KVT, hs = sec == 0 ? QHS : KHS; const rsrc_t img = rsrc(ws + (sec == 0 ? WS_QI : WS_KV) + (size_t)hb * hs + tile32 * tb, 7 * hs + tb); const float sc = sec == 0 ? QSCALE : 1.f;
        f32x4 cc[9]; const f32x2 s0 = stt[n], s1 = stt[16 + n];
#pragma unroll
        for (int lt = 0; lt < 9; ++lt) cc[lt] = *(const LAS f32x4*)(cwt + (lt < 8 ? 36 * (lt >> 1) + 4 * (lt & 1) + (g & 1) * 8 + (g >> 1) * 20 : 36 * g + 16));
        LAS unsigned* xt = (LAS unsigned*)(lds + wv * 1024);
#pragma unroll
        for (int T = 0; T < 2; ++T) { const f32x2 st = T ? s1 : s0; const float r = st[1] * sc;
            const int pos = sec == 0 ? 16 * T + n : ((n >> 3) << 4) | (T << 3) | (n & 7); float e[4];
#pragma unroll
            for (int i = 0; i < 4; ++i) e[i] = r * (acc[8][T][i] - st[0] * cc[8][i]);
            xt[pos * 4 + g] = pk2(e[0], e[1]); xt[128 + pos * 4 + g] = pk2(e[2], e[3]);
#pragma unroll
            for (int pl = 0; pl < 4; ++pl) { float v[8];
#pragma unroll
                for (int i = 0; i < 4; ++i) { v[i] = r * (acc[2 * pl][T][i] - st[0] * cc[2 * pl][i]); v[4 + i] = r * (acc[2 * pl + 1][T][i] - st[0] * cc[2 * pl + 1][i]); }
                st128(img, (g >> 1) * hs + (g & 1) * 512 + pos * 16, 2 * pl * hs, pk8(v)); } }
        const u32x4 xe = *(const LAS u32x4*)(xt + lane * 4);
#pragma unroll
        for (int k = 0; k < 4; ++k) st32(img, (lane >> 5) * hs + 1024 + (lane & 31) * 4, 2 * k * hs, xe[k]);
    }
}

DI int dslot(int ct, int m) { const int G = ct / 3, t = ct - 3 * G; return 48 * G + 12 * (m >> 2) + 4 * t + (m & 3); }

template <int CTRL> DI float dpp_add(float v) { return v + __builtin_bit_cast(float, __builtin_amdgcn_update_dpp(0, __builtin_bit_cast(int, v), CTRL, 0xF, 0xF, true)); }
DI f32x2 up2(unsigned u) { return f32x2{bflo(u), bfhi(u)}; }
DI void p1_tables(const char* x, const char* w, char* ws, lptr lds, int hb, int lane, int wv) {
    const rsrc_t rx = rsrc(x + (size_t)(hb * 32 + wv * 8) * 1152, 8 * 1152), rt = rsrc(ws + WS_T1, T1_BYTES); u32x4 xv[9], v[9];
    const int t = wv * 64 + lane, rs = (t * 3641) >> 16, ch = t - rs * 18, cg = hb / 5, part = hb - cg * 5;
    const rsrc_t rw = rsrc(w + (size_t)part * 116 * 3456 + cg * 288, hb < 60 ? ((part < 4 ? 116 : 112) - 1) * 3456 + 288 : 0); const unsigned vo = rs < 14 ? rs * 3456 + ch * 16 : OOB;
#pragma unroll
    for (int k = 0; k < 9; ++k) xv[k] = __builtin_amdgcn_raw_buffer_load_b128(rx, (lane >> 3) * 1152 + (lane & 7) * 16, k * 128, 0);
#pragma unroll
    for (int k = 0; k < 9; ++k) v[k] = __builtin_amdgcn_raw_buffer_load_b128(rw, vo, k * 14 * 3456, 0);
    f32x2 a1[2] = {}, a2[2] = {}, cs[4] = {};
#pragma unroll
    for (int k = 0; k < 36; ++k) { const f32x2 u = up2(xv[k >> 2][k & 3]); a1[k & 1] += u; a2[k & 1] += u * u; }
    float s1 = (a1[0][0] + a1[0][1]) + (a1[1][0] + a1[1][1]), s2 = (a2[0][0] + a2[0][1]) + (a2[1][0] + a2[1][1]);
    s1 = dpp_add<0x141>(dpp_add<0x4E>(dpp_add<0xB1>(s1))); s2 = dpp_add<0x141>(dpp_add<0x4E>(dpp_add<0xB1>(s2)));
    const float mean = s1 * (1.f / D), rstd = __builtin_amdgcn_rsqf(fmaxf(s2 * (1.f / D) - mean * mean, 0.f) + LN_EPS);
    st64(rt, lane & 7 ? OOB : (lane >> 3) * 8, (hb * 32 + wv * 8) * 8, u32x2{__float_as_uint(mean), __float_as_uint(rstd)});
#pragma unroll
    for (int k = 0; k < 36; ++k) cs[k & 3] += up2(v[k >> 2][k & 3]);
    *(LAS f32x4*)(lds + t * 32) = f32x4{cs[0][0], cs[0][1], cs[1][0], cs[1][1]}; *(LAS f32x4*)(lds + t * 32 + 16) = f32x4{cs[2][0], cs[2][1], cs[3][0], cs[3][1]};
    lbar();
    if (wv < 3) { const LAS float* pc = (const LAS float*)lds + t; float s = 0.f;
#pragma unroll
        for (int k = 0; k < 14; ++k) s += pc[k * 144];
        st32(rt, t < 144 ? t * 4 : OOB, 16384 + hb * 1024, __float_as_uint(s)); }
}
DI void p1_wo(const unsigned short* wo, char* ws, int hb, int lane, int wv) {
    const int n = lane & 15, g = lane >> 4; const auto rw = rsrc(wo, D * D * 2); unsigned short e[3][8];
#pragma unroll
    for (int i = 0; i < 3; ++i) { const int fr = hb * 4 + wv + 256 * i, ct = fr / 18, ks = fr - ct * 18, col = dslot(ct, n);
        const int r0 = ks < 16 ? 18 * (2 * ks + (g >> 1)) + 2 * (g >> 1) + 4 * (g & 1) : 18 * (16 * (ks - 16) + 4 * g);
        const unsigned vo = fr < 648 ? (r0 * D + col) * 2 : OOB;
#pragma unroll
        for (int j = 0; j < 8; ++j) e[i][j] = __builtin_amdgcn_raw_buffer_load_b16(rw, vo, (ks < 16 ? 8 * (j >> 2) + (j & 3) : 18 * (j >> 1) + (j & 2 ? 0 : 16) + (j & 1)) * (D * 2), 0); }
#pragma unroll
    for (int i = 0; i < 3; ++i) { const int fr = hb * 4 + wv + 256 * i;
        st128(rsrc(ws + WS_WO, 648 * 1024), lane * 16, fr * 1024, u32x4{e[i][0] | (unsigned)e[i][1] << 16, e[i][2] | (unsigned)e[i][3] << 16, e[i][4] | (unsigned)e[i][5] << 16, e[i][6] | (unsigned)e[i][7] << 16}); }
    if (wv == 0) st128(rsrc(ws + WS_ST, 65536), lane * 16, hb * 1024, u32x4{SENT, SENT, SENT, SENT});
}

DI float lo_half(float v) { return __uint_as_float(__builtin_amdgcn_permlane32_swap(__float_as_uint(v), __float_as_uint(v), false, false)[0]); }
DI void p2_redo(char* ws, int head, int qt, int lane) {
    const int r = lane & 31, hh = lane >> 5; f32x16 o;
    {
        const auto rq = rsrc(ws + WS_QI + (size_t)head * QHS + qt * QT, QT), rk = rsrc(ws + WS_KV + (size_t)head * KHS, 64 * KVT);
        const unsigned ka = hh * 512 + r * 16, xa = hh ? OOB : 1024 + r * 4, va = r < 19 ? 1152 + hh * 320 + r * 16 : OOB;
        const bf16x8 q1 = __builtin_bit_cast(bf16x8, ld128(rq, ka, 0)), q2 = __builtin_bit_cast(bf16x8, u32x4{ld32(rq, xa, 0), 0u, 0u, 0u});
        o = f32x16{}; float m = -3.0e38f;
#pragma unroll 1
        for (int t = 0; t < 64; ++t) {
            const u32x4 kf = ld128(rk, ka, t * KVT), v0 = ld128(rk, va, t * KVT), v1 = ld128(rk, va + 640, t * KVT); const unsigned kx = ld32(rk, xa, t * KVT);
            f32x16 s = MF32(__builtin_bit_cast(bf16x8, kf), q1, f32x16{});
            s = MF32(__builtin_bit_cast(bf16x8, u32x4{kx, 0u, 0u, 0u}), q2, s);
            float mt = s[0];
#pragma unroll
            for (int i = 1; i < 16; ++i) mt = fmaxf(mt, s[i]);
            mt = fmaxf(mt, __shfl_xor(mt, 32)); const float mn = fmaxf(m, mt), al = __builtin_amdgcn_exp2f(m - mn); m = mn;
#pragma unroll
            for (int i = 0; i < 16; ++i) { s[i] = __builtin_amdgcn_exp2f(s[i] - mn); o[i] *= al; }
            o = MF32(__builtin_bit_cast(bf16x8, v0), __builtin_bit_cast(bf16x8, u32x4{pk2(s[0], s[1]), pk2(s[2], s[3]), pk2(s[4], s[5]), pk2(s[6], s[7])}), o);
            o = MF32(__builtin_bit_cast(bf16x8, v1), __builtin_bit_cast(bf16x8, u32x4{pk2(s[8], s[9]), pk2(s[10], s[11]), pk2(s[12], s[13]), pk2(s[14], s[15])}), o);
        }
    }
    const float il = __builtin_amdgcn_rcpf(lo_half(o[10]));
    const rsrc_t oi = rsrc(ws + WS_OI + (size_t)qt * ORS, 36864); const unsigned vo = (r >> 4) * 18432 + (r & 15) * 16;
    st128(oi, vo + hh * 256, (head >> 1) * 1024 + (head & 1) * 512, u32x4{pk2(o[0] * il, o[1] * il), pk2(o[2] * il, o[3] * il), pk2(o[4] * il, o[5] * il), pk2(o[6] * il, o[7] * il)});
    st32(oi, hh ? OOB : (r >> 4) * 18432 + (r & 15) * 4, 16384 + head * 64, pk2(o[8] * il, o[9] * il));
}

DI void p2_compute(char* ws, lptr lds, int head, int qb, int lane, int wv, const char* w1, const char* w2) {
    const int r = lane & 31, hh = lane >> 5, kh = wv & 1, q0 = qb * 8 + (wv >> 1) * 4;
    const u32x4 sq = srd(ws + WS_QI + (size_t)head * QHS + q0 * QT, 4 * QT), sk = srd(ws + WS_KV + (size_t)head * KHS, 64 * KVT);
    const u32x4 qo = {(unsigned)(((2 * kh + 2) & 3) * QT), (unsigned)(((2 * kh + 3) & 3) * QT), (unsigned)(2 * kh * QT), (unsigned)((2 * kh + 1) * QT)};
    const unsigned lb = (unsigned)(size_t)lds + kh * 32 * KVT, qa = hh * 512 + r * 16, qx = hh ? OOB : r * 4;
    unsigned ka = lb + qa, xa = lb + 1024 + r * 4 + (hh ? LOOB : 0), va = lb + 1152 + hh * 320 + r * 16 + (r < 19 ? 0 : LOOB);
    auto run = [&](int i) { const int q = blockIdx.x + 256 * (wv + 4 * i); return (unsigned)(q >= 2304 ? q - 2304 : q) * 8192; };
    unsigned tm = 0, mask = TMASK(wv), r0 = run(0), r1 = run(1), r2 = run(2), r3 = run(3); u32x4 st = srd(w1, S * F * 2);
#pragma unroll
    for (int i = 0; i < 5; ++i) tm |= (blockIdx.x + 256 * (wv + 4 * i) < 2304) << i;
    const unsigned om = (head >> 1) * 1024 + (head & 1) * 512, ox = 16384 + head * 64, l0 = (unsigned)(size_t)lds;
    asm volatile(ATT_ASM : "+{v168}"(ka), "+{v169}"(xa), "+{v170}"(va), "+{s53}"(tm), "+{s52}"(mask), "+{s[64:67]}"(st), "+{s54}"(r0), "+{s57}"(r1), "+{s61}"(r2), "+{s62}"(r3)
                 : "{v171}"(qa), "{v172}"(qx), "{v173}"(lane * 16), "{v174}"(lane * 128), "{v175}"(l0 + wv * 6144 + lane * 16), "{v176}"(l0 + (wv ^ 1) * 6144 + lane * 16),
                   "{v177}"((r >> 4) * 18432 + (r & 15) * 16 + hh * 256), "{v178}"(hh ? OOB : (r >> 4) * 18432 + (r & 15) * 4),
                   "{s63}"(run(4)), "{s55}"(l0 + L_JUNK), "{s[68:69]}"((u64)w1 & 0xFFFFFFFFFFFFull), "{s[70:71]}"((u64)w2 & 0xFFFFFFFFFFFFull),
                   "{s[40:43]}"(sk), "{s[44:47]}"(sq), "{s[48:51]}"(qo), "{s56}"(l0 + wv * 1024), "{s58}"(wv * 1024),
                   "{s[72:75]}"(srd(ws + WS_OI + (size_t)(q0 + 2 * kh) * ORS, ORS + 36864)), "{s[76:79]}"(u32x4{om, ox, om + ORS, ox + ORS}) : ATT_CLOB);
    if (__builtin_expect(tm == 0, 0)) {
        asm volatile("s_waitcnt vmcnt(0)" ::: "memory");
#pragma unroll 1
        for (int a = 0; a < 2; ++a) p2_redo(ws, head, q0 + 2 * kh + a, fresh_lane());
    }
}

DI unsigned w_voff(int par, int lane, int wrow, int nt) { const int row = 4 * par + (lane >> 4), sl = lane & 15; return (unsigned)(row * wrow + nt * 256 + ((((sl >> 1) ^ row) << 5) | ((sl & 1) << 4))); }
DI void touch1(u32x4 sr, unsigned vo, unsigned so, unsigned junk) { asm volatile("s_mov_b32 m0, %3\n\ts_nop 0\n\tbuffer_load_dword %0, %1, %2 offen lds" :: "v"(vo), "s"(sr), "s"(so), "s"(junk) : "memory"); }
DI u32x4 code_srd() {
    u64 pc; unsigned n, back;
    asm volatile("s_getpc_b64 %0\nLkernel_here:\n\ts_mov_b32 %1, Lkernel_end-Lkernel_start\n\ts_mov_b32 %2, Lkernel_here-Lkernel_start" : "=s"(pc), "=s"(n), "=s"(back));
    return srd((const void*)(pc - back), n);
}

DI float swap_add32(float a, float b) { const auto r = __builtin_amdgcn_permlane32_swap(__float_as_uint(a), __float_as_uint(b), false, false); return __uint_as_float(r[0]) + __uint_as_float(r[1]); }
DI float swap_add16(float a, float b) { const auto r = __builtin_amdgcn_permlane16_swap(__float_as_uint(a), __float_as_uint(b), false, false); return __uint_as_float(r[0]) + __uint_as_float(r[1]); }
DI LAS u32x2* p3_px(lptr lds, int c, int T, int lane) { const int G = (c * 43) >> 7; return (LAS u32x2*)(lds + L_X + (16 * T + (lane & 15)) * 288 + 96 * G + 24 * (lane >> 4) + 8 * (c - 3 * G)); }
DI void p3_h(const f32x4& a, u32x2 xv, LAS u32x2* px, float& s1, float& s2) {
    const u32x2 hv = {pk2(a[0] + bflo(xv[0]), a[1] + bfhi(xv[0])), pk2(a[2] + bflo(xv[1]), a[3] + bfhi(xv[1]))};
    *px = hv;
    const float h0 = bflo(hv[0]), h1 = bfhi(hv[0]), h2 = bflo(hv[1]), h3 = bfhi(hv[1]);
    s1 += (h0 + h1) + (h2 + h3); s2 += (h0 * h0 + h1 * h1) + (h2 * h2 + h3 * h3);
}
DI void p3_part(const float (&s1)[2], const float (&s2)[2], lptr lds, int wv, int lane) { *(LAS float*)(lds + L_PART + wv * 256 + lane * 4) = swap_add16(swap_add32(s1[0], s1[1]), swap_add32(s2[0], s2[1])); }
DI void p3_ht(float (&hv)[8], lptr lds, int c, int lane) {
    const int G = (c * 43) >> 7;
#pragma unroll
    for (int T = 0; T < 2; ++T) { const u32x2 v = __builtin_bit_cast(u32x2, ldstr(lds + L_X + (16 * T + 4 * (lane >> 4) + ((lane & 15) >> 2)) * 288 + 96 * G + 24 * (lane & 3) + 8 * (c - 3 * G)));
        hv[4 * T] = bflo(v[0]); hv[4 * T + 1] = bfhi(v[0]); hv[4 * T + 2] = bflo(v[1]); hv[4 * T + 3] = bfhi(v[1]); }
}
DI void p3_norm(const float (&hv)[8], char* ws, lptr lds, int ct, int rg, int lane) {
    const LAS f32x4* stat = (const LAS f32x4*)(lds + L_STAT) + 2 * (lane >> 4); float z[8];
#pragma unroll
    for (int k = 0; k < 4; ++k) { const f32x4 s = stat[(k >> 1) * 8 + (k & 1)]; z[2 * k] = (hv[2 * k] - s[0]) * s[1]; z[2 * k + 1] = (hv[2 * k + 1] - s[2]) * s[3]; }
    st128(rsrc(ws + WS_HN, 36 * HNS), lane * 16, ct * HNS + rg * 1024, pk8(z));
}
DI void p3_hcopy(char* ws, lptr lds, int rg, int cq, int piece, int lane) {
    st128(rsrc(ws + WS_HR, S * D * 2), lane * 16, (rg * 4 + cq) * 9216 + piece * 1024, *(const LAS u32x4*)(lds + L_X + piece * 1024 + lane * 16));
}

DI void ring_fill(u32x4 sw, int wrow, int nt, int s0, unsigned ring, int lane, int wv) {
    const unsigned a0 = w_voff(0, lane, wrow, nt) + 8 * wv * wrow, a1 = w_voff(1, lane, wrow, nt) + 8 * wv * wrow - 1024;
#pragma unroll 1
    for (int t = 0; t < 6; ++t)
        asm volatile("s_mov_b32 m0, %4\n\ts_nop 0\n\tbuffer_load_dwordx4 %0, %2, %3 offen lds\n\tbuffer_load_dwordx4 %1, %2, %3 offen offset:1024 lds"
                     :: "v"(a0), "v"(a1), "s"(sw), "s"(t * 32 * wrow), "s"(ring + ((s0 + t) & 7) * 8192 + wv * 2048) : "memory");
}
DI void p3_compute(char* ws, lptr lds, int rg, int cq, int lane, int wv, u32x4 sw, int wrow, int nt, int s0) {
    const unsigned l0 = (unsigned)(size_t)lds; f32x16 r; unsigned so = wv * 1024; u32x2 xv[4];
#pragma unroll
    for (int k = 0; k < 4; ++k) xv[k] = *p3_px(lds, wv + 4 * (k >> 1), k & 1, lane);
    asm volatile(P3_ASM2 : "={v[144:159]}"(r), "+{s58}"(so)
                 : "{v192}"(lane * 16), "{v193}"(l0 + L_O + lane * 16), "{v194}"(l0 + L_O + 16384 + (lane >> 4) * 256 + (lane & 15) * 4), "{s[40:43]}"(srd(ws + WS_WO + (size_t)cq * 9 * 18432, 9 * 18432)),
                   "{s[44:47]}"(srd(ws + WS_OI + (size_t)rg * ORS, 36864)), "{s56}"(wv * 18432), "{s59}"(l0 + L_O + wv * 1024) : P3_CLOB2);
    float s1[2] = {}, s2[2] = {}, ha[8], hb[8];
#pragma unroll
    for (int k = 0; k < 4; ++k) p3_h(f32x4{r[4 * k], r[4 * k + 1], r[4 * k + 2], r[4 * k + 3]}, xv[k], p3_px(lds, wv + 4 * (k >> 1), k & 1, lane), s1[k & 1], s2[k & 1]);
    p3_part(s1, s2, lds, wv, lane);
    lbar();
    ring_fill(sw, wrow, nt, s0, l0 + (s0 ? L_RING2 : L_RING), lane, wv);
    p3_hcopy(ws, lds, rg, cq, wv, lane); p3_hcopy(ws, lds, rg, cq, wv + 4, lane); if (wv == 3) p3_hcopy(ws, lds, rg, cq, 8, lane);
    p3_ht(ha, lds, wv, lane); p3_ht(hb, lds, wv + 4, lane);
    lbar();
    p3_norm(ha, ws, lds, cq * 9 + wv, rg, lane); p3_norm(hb, ws, lds, cq * 9 + wv + 4, rg, lane);
}

struct WoTile { u32x32 w0, w1; u32x8 w2; u32x32 x0; u32x4 x1; };
DI void p3_service(const WoTile& w, char* ws, lptr lds, int watch, int rg, int cq, int lane, u64 tag) {
    const unsigned l0 = (unsigned)(size_t)lds; const u32x2 xv[2] = {*p3_px(lds, 8, 0, lane), *p3_px(lds, 8, 1, lane)};
    poll<D2>(ws, 1, watch, tag);
    wait_bar<0>();
    f32x8 r;
    asm volatile(P3_ASM1 : "={v[144:151]}"(r)
                 : "{v[0:31]}"(w.w0), "{v[32:63]}"(w.w1), "{v[64:71]}"(w.w2), "{v193}"(l0 + L_O + lane * 16), "{v194}"(l0 + L_O + 16384 + (lane >> 4) * 256 + (lane & 15) * 4) : P3_CLOB1);
    float s1[2] = {}, s2[2] = {}, h8[8];
#pragma unroll
    for (int T = 0; T < 2; ++T) p3_h(f32x4{r[4 * T], r[4 * T + 1], r[4 * T + 2], r[4 * T + 3]}, xv[T], p3_px(lds, 8, T, lane), s1[T], s2[T]);
    p3_part(s1, s2, lds, 4, lane);
    lbar();
    const LAS float* pp = (const LAS float*)(lds + L_PART) + lane; const rsrc_t st = rsrc(ws + WS_ST + (size_t)rg * 1024, 1024);
    const float mine = ((pp[0] + pp[64]) + (pp[128] + pp[192])) + pp[256];
    st32(st, lane * 4, cq * 256, nosent(__float_as_uint(mine)));
    __builtin_amdgcn_s_sleep(DS);
    p3_ht(h8, lds, 8, lane);
    unsigned e[3];
#pragma unroll 1
    for (int sp = 0; sp < SPIN_CAP; ++sp) { fence();
#pragma unroll
        for (int k = 0; k < 3; ++k) e[k] = ld32(st, lane * 4, ((cq + 1 + k) & 3) * 256);
        if (__all(e[0] != SENT && e[1] != SENT && e[2] != SENT)) break; }
    const float tot = (mine + __uint_as_float(e[1])) + (__uint_as_float(e[0]) + __uint_as_float(e[2]));
    const auto ex = __builtin_amdgcn_permlane16_swap(__float_as_uint(tot), __float_as_uint(tot), false, false);
    const float mean = __uint_as_float(ex[0]) * (1.f / D), var = fmaxf(__uint_as_float(ex[1]) * (1.f / D) - mean * mean, 0.f);
    const float ra = __builtin_amdgcn_rsqf(var + LN_EPS), rb = __builtin_amdgcn_rsqf(var * ra * ra + LN_EPS);
    ((LAS f32x2*)(lds + L_STAT))[(lane >> 5) * 16 + (lane & 15)] = f32x2{mean, ra * rb};
    lbar();
    p3_norm(h8, ws, lds, cq * 9 + 8, rg, lane);
}

DI f32x2 gelu2(f32x2 u) {
    constexpr float c1 = -2.f * 0.7978845608028654f * 1.4426950408889634f, c2 = c1 * 0.044715f;
    const f32x2 z = u * (u * u * c2 + c1), d = f32x2{__builtin_amdgcn_exp2f(z[0]), __builtin_amdgcn_exp2f(z[1])} + 1.f;
    return u * f32x2{__builtin_amdgcn_rcpf(d[0]), __builtin_amdgcn_rcpf(d[1])};
}
DI u32x8 tr_addr(unsigned l0, int rot, int lane) { const int row = 4 * (lane >> 4) + ((lane & 15) >> 2); u32x8 ta;
#pragma unroll
    for (int i = 0; i < 8; ++i) ta[i] = l0 + row * 256 + ((((i + rot) & 7) ^ (row & 7)) << 5) + 8 * (lane & 3);
    return ta; }

DI void p4_compute(const char* b1, char* ws, unsigned l0, int kh, int nt, int mt, int lane, int wv, u32x4 sw, u32x4 sw2, int nt2) {
    f32x32 acc[3]; f32x16 bias;
    { const rsrc_t rb = rsrc(b1 + (nt * 128 + 64 * kh) * 2, 128);
#pragma unroll
      for (int k = 0; k < 4; ++k) { const u32x2 v = __builtin_amdgcn_raw_buffer_load_b64(rb, (lane >> 4) * 8, 32 * k, 0); bias[4 * k] = bflo(v[0]); bias[4 * k + 1] = bfhi(v[0]); bias[4 * k + 2] = bflo(v[1]); bias[4 * k + 3] = bfhi(v[1]); } }
    const char* ab = ws + WS_HN + (size_t)(12 * mt + 3 * wv) * HNS + 32 * kh * 1024; char* ex = ws + WS_E1 + (size_t)(nt * 3 + mt) * E1S + wv * 6144; unsigned so = 6 * 32 * 9216;
    asm volatile(GEMM1_ASM : "={v[0:31]}"(acc[0]), "={v[32:63]}"(acc[1]), "={v[64:95]}"(acc[2]), "+{s58}"(so)
                 : "{v[224:231]}"(tr_addr(l0 + L_RING, 4 * kh + 4, lane)), "{v232}"(lane * 16), "{v[238:253]}"(bias), "{v[234:235]}"(u32x2{w_voff(0, lane, 9216, nt) + 8 * wv * 9216, w_voff(1, lane, 9216, nt) + 8 * wv * 9216 - 1024}),
                   "{s[40:43]}"(sw), "{s[44:47]}"(srd(ab, 32768)), "{s[48:51]}"(srd(ab + HNS, 32768)), "{s[52:55]}"(srd(ab + 2 * HNS, 32768)),
                   "{s56}"(l0 + L_RING + wv * 2048), "{s61}"(32 * 9216), "{s[64:67]}"(srd(ex + (1 - kh) * 24576, 6144)), "{s68}"(4096),
                   "{v[236:237]}"(u32x2{w_voff(0, lane, 4096, nt2) + 8 * wv * 4096, w_voff(1, lane, 4096, nt2) + 8 * wv * 4096 - 1024}), "{s[72:75]}"(sw2), "{s69}"(l0 + L_RING2 + wv * 2048) : GEMM_CLOB);
    lane = fresh_lane();
    const rsrc_t rx = rsrc(ex + kh * 24576, 6144);
    __builtin_amdgcn_s_sleep(DE1);
    u32x4 r[6];
#pragma unroll 1
    for (int sp = 0; sp < SPIN_CAP; ++sp) { fence(); unsigned m = 0;
#pragma unroll
        for (int k = 0; k < 6; ++k) r[k] = ld128(rx, lane * 16, k * 1024);
#pragma unroll
        for (int k = 0; k < 6; ++k) m = max(max(m, max(r[k][0], r[k][1])), max(r[k][2], r[k][3]));
        if (__all(m != SENT)) break; }
    const rsrc_t ru = rsrc(ws + WS_UI + (size_t)(12 * mt + 3 * wv) * UIS + (nt * 4 + 2 * kh) * 1024, 2 * UIS + 2048);
#pragma unroll
    for (int k = 0; k < 6; ++k) { const f32x32& a = acc[k >> 1]; const int o = 16 + 8 * (k & 1); u32x4 w;
#pragma unroll
        for (int e = 0; e < 4; ++e) { const f32x2 y = gelu2(f32x2{a[o + 2 * e], a[o + 2 * e + 1]} + up2(r[k][e])); w[e] = pk2(y[0], y[1]); }
        st128(ru, lane * 16, (k >> 1) * UIS + (k & 1) * 1024, w); }
}

DI void p5_compute(const char* b2, char* ws, char* out, unsigned l0, int j5, int tl, int k0, int nk, int lane, int wv, u32x4 sw) {
    const int nt = tl / 3, mt = tl - nt * 3, n = lane & 15, g = lane >> 4, rot = j5 < 3 ? 2 * j5 + 2 : j5 == 3 ? 7 : 0; const bool keep6 = j5 < 3;
    f32x32 acc[3];
    const char* ab = ws + WS_UI + (size_t)(12 * mt + 3 * wv) * UIS + k0 * 1024; unsigned so = 6 * 32 * 4096;
    char* exb = ws + WS_E2 + (size_t)tl * E2S + wv * 1536;
    auto dst = [&](int i) { const int c = (i + rot) & 7, own = c < 6 ? c >> 1 : c - 3; return (unsigned)((c * 4 + (j5 - own + 4) % 5) * 6144); };
    asm volatile(GEMM2_ASM : "={v[0:31]}"(acc[0]), "={v[32:63]}"(acc[1]), "={v[64:95]}"(acc[2]), "+{s58}"(so)
                 : "{v[224:231]}"(tr_addr(l0 + L_RING2, rot, lane)), "{v232}"(lane * 16), "{v233}"(lane * 8 + 1024),
                   "{v[234:235]}"(u32x2{w_voff(0, lane, 4096, nt) + 8 * wv * 4096, w_voff(1, lane, 4096, nt) + 8 * wv * 4096 - 1024}),
                   "{s[40:43]}"(sw), "{s[44:47]}"(srd(ab, nk * 1024)), "{s[48:51]}"(srd(ab + UIS, nk * 1024)), "{s[52:55]}"(srd(ab + 2 * UIS, nk * 1024)),
                   "{s56}"(l0 + L_RING2 + wv * 2048), "{s61}"(32 * 4096), "{s[64:67]}"(srd(exb, 196608 - wv * 1536)),
                   "{s[68:71]}"(u32x4{dst(0), dst(1), dst(2), dst(3)}), "{s[72:73]}"(u32x2{dst(4), dst(5)}) : GEMM_CLOB);
    const rsrc_t ex = rsrc(exb, 196608 - wv * 1536);
    if (!keep6) { const unsigned d6 = dst(6);
        st128(ex, lane * 16, d6, u32x4{nosent(pk2(acc[0][24], acc[0][25])), nosent(pk2(acc[0][26], acc[0][27])), nosent(pk2(acc[1][24], acc[1][25])), nosent(pk2(acc[1][26], acc[1][27]))});
        st64(ex, lane * 8 + 1024, d6, u32x2{nosent(pk2(acc[2][24], acc[2][25])), nosent(pk2(acc[2][26], acc[2][27]))}); }
    const int s6 = nt * 128 + 16 * ((6 + rot) & 7) + n, s7 = nt * 128 + 16 * ((7 + rot) & 7) + n; const unsigned co = (192 * mt + 48 * wv + 12 * g) * 2;
    const u32x2 ro = {keep6 ? s6 * 1152 + co : OOB, s7 * 1152 + co};
    const rsrc_t rb = rsrc(b2, S * 2); const float b6 = bflo(__builtin_amdgcn_raw_buffer_load_b16(rb, s6 * 2, 0, 0)), b7 = bflo(__builtin_amdgcn_raw_buffer_load_b16(rb, s7 * 2, 0, 0));
    const rsrc_t rh = rsrc(ws + WS_HR, S * D * 2); u32x3 hr[4];
#pragma unroll
    for (int k = 0; k < 4; ++k) { const int s = k < 2 ? s6 : s7, G = 4 * mt + wv, cq = (G * 43) >> 7;
        hr[k] = __builtin_amdgcn_raw_buffer_load_b96(rh, ((s >> 5) * 4 + cq) * 9216 + (s & 31) * 288 + (G - 3 * cq) * 96 + 24 * g + 12 * (k & 1), 0, 16); }
    const unsigned u6 = keep6 ? ((6 + rot) & 7) * 24576 : OOB, u7 = ((7 + rot) & 7) * 24576;
    u32x4 ra[8] = {}; u32x2 rc[8] = {}; f32x2 y[2][6]; const u32x4 sx = srd(exb, 196608 - wv * 1536); unsigned tmp;
#define E2_GET(MASK, MASK2, TAIL) asm volatile(E2_FETCH TAIL "s_waitcnt vmcnt(0)" : "+v"(ra[0]), "+v"(ra[1]), "+v"(ra[2]), "+v"(ra[3]), "+v"(ra[4]), "+v"(ra[5]), "+v"(ra[6]), "+v"(ra[7]), "+v"(rc[0]), "+v"(rc[1]), "+v"(rc[2]), "+v"(rc[3]), \
        "+v"(rc[4]), "+v"(rc[5]), "+v"(rc[6]), "+v"(rc[7]), "=&s"(tmp) : "v"(lane * 16), "v"(lane * 8 + 1024), "s"(sx), "s"(MASK), "s"(u6), "s"(u7), "s"(MASK2) : "scc", "memory")
    const unsigned all = keep6 ? 0xFF : 0xF0, early = (j5 == 0 ? 0xCE : j5 == 1 ? 0x8C : j5 == 2 ? 0x8C : j5 == 3 ? 0xC0 : 0xC0);
    __builtin_amdgcn_s_sleep(DE2A);
    E2_GET(early, all & ~early, "s_sleep 26\n" E2_FETCH_LATE);
#pragma unroll
    for (int u = 0; u < 2; ++u)
#pragma unroll
        for (int e = 0; e < 6; ++e) { const int o = 24 + 4 * u + 2 * (e & 1); y[u][e] = f32x2{acc[e >> 1][o], acc[e >> 1][o + 1]} + (u ? b7 : b6) + up2(hr[2 * u + e / 3][e % 3]); }
#pragma unroll 1
    for (int sp = 0; sp < SPIN_CAP; ++sp) { unsigned m[8], miss = 0;
#pragma unroll
        for (int k = 0; k < 8; ++k) m[k] = max(max(max(ra[k][0], ra[k][1]), max(ra[k][2], ra[k][3])), max(rc[k][0], rc[k][1]));
        if (!__any(max(max(max(m[0], m[1]), max(m[2], m[3])), max(max(m[4], m[5]), max(m[6], m[7]))) == SENT)) break;
#pragma unroll
        for (int k = 0; k < 8; ++k) if (__any(m[k] == SENT)) miss |= 1u << k;
        E2_GET(miss, 0, ""); }
    const rsrc_t ro_ = rsrc(out, S * D * 2);
#pragma unroll
    for (int u = 0; u < 2; ++u) { unsigned w[6];
#pragma unroll
        for (int e = 0; e < 6; ++e) { const auto pt = [&](int k) { return up2(e < 4 ? ra[4 * u + k][e] : rc[4 * u + k][e - 4]); };
            const f32x2 v = (y[u][e] + pt(0)) + (pt(1) + pt(2)) + pt(3); w[e] = pk2(v[0], v[1]); }
        __builtin_amdgcn_raw_buffer_store_b96(u32x3{w[0], w[1], w[2]}, ro_, ro[u], 0, 0); __builtin_amdgcn_raw_buffer_store_b96(u32x3{w[3], w[4], w[5]}, ro_, ro[u] + 12, 0, 0); }
}

__global__ __launch_bounds__(320, 2) void fused_block(const char* x, const char* wqkv, const unsigned short* wo, const char* w1, const char* b1, const char* w2, const char* b2, char* ws, char* out) {
    extern __shared__ __attribute__((aligned(16))) char smem[];
    const lptr lds = (lptr)smem; const unsigned l0 = (unsigned)(size_t)lds;
    const int lane = threadIdx.x & 63, wv = rfl(threadIdx.x >> 6), b = blockIdx.x, xc = b & 7, j = b >> 3;
    asm volatile("Lkernel_start:");
    if (wv == 4) {
        const u32x4 code = code_srd(); const unsigned vc = lane * 128;
        if (j >= 24) touch1(code, vc, (j & 7) * 8192, l0 + L_JUNK);
        const u32x2 ep = ld64(rsrc(ws + WS_EP, 8), 0, 0);
        const bool has1 = j < 27, has2 = j < 30;
        const int kh = xc >> 2, nt1 = (xc & 3) * 9 + j / 3, mt1 = j % 3;
        const int wl = xc * 30 + j, j5 = wl / 48, tl = wl - j5 * 48, nt2 = tl / 3, mt2 = tl - nt2 * 3, k0 = 29 * j5, nk2 = j5 < 4 ? 29 : 28;
        const int head = xc * 4 + (j >> 3), qb = j & 7;
        const u32x4 sw1 = srd(w1 + (size_t)kh * 1024 * 9216, 1024 * 9216), sw2 = srd(w2 + (size_t)k0 * 32 * 4096, has2 ? nk2 * 32 * 4096 : 0);
        const int rg = xc * 8 + (j >> 2), cq = j & 3; u32x8 xo; unsigned xo8, sl = lane * 16; u32x4 sent = {SENT, SENT, SENT, SENT}; int hw = 192 + lane;
#pragma unroll
        for (int i = 0; i < 9; ++i) { const int c = i * 64 + lane, row = (c * 3641) >> 16; const unsigned o = row * 1152 + (c - row * 18) * 16; if (i < 8) xo[i] = o; else xo8 = o; }
        int w1st, w2nd, w3rd, w4th = -1;
        { const int rt = lane & 15, cg = (lane >> 4) * 4 + (xc >> 1); w1st = lane < 48 ? (((rt & 3) * 6 + cg % 6) << 3) | ((rt >> 2) * 2 + cg / 6) : -1; }
        w2nd = lane < 32 ? (((lane & 3) * 8 + xc) << 3) | (lane >> 2) : -1;
        { const int r2 = 32 * kh + (lane >> 1); w3rd = ((((r2 & 7) << 2) | (mt1 + (lane & 1))) << 3) | (r2 >> 3); }
        { const int pr = (k0 >> 1) + lane, nt = pr >> 1; if (2 * pr < k0 + nk2) w4th = (((nt % 9) * 3 + mt2) << 3) | ((pr & 1) * 4 + nt / 9); }
        asm volatile("" : "+v"(xo), "+v"(xo8), "+v"(sl), "+v"(sent), "+v"(hw), "+v"(w1st), "+v"(w2nd), "+v"(w3rd), "+v"(w4th));
#pragma unroll 1
        for (int i = j >= 24 ? 6 : 0; i < 8; ++i) asm volatile("s_barrier" ::: "memory");
        const u64 tag = (rfl(ep[0]) | (u64)rfl(ep[1]) << 32) + 1;
        if (j < 24) {
        const int rt = (xc >> 1) * 4 + j / 6, cg = (xc & 1) * 6 + j % 6; const rsrc_t r1 = rsrc(ws + WS_T1, T1_BYTES);
        poll(ws, 4, lane < 4 ? 192 + 4 * rt + lane : lane < 9 ? 192 + 5 * cg + lane - 4 : -1, tag);
        u32x4 c[5]; const u32x4 sv = ld128(r1, lane * 16, rt * 1024);
#pragma unroll
        for (int k = 0; k < 5; ++k) c[k] = ld128(r1, lane < 36 ? lane * 16 : OOB, 16384 + (cg * 5 + k) * 1024);
        asm volatile("s_barrier" ::: "memory");
        *(LAS f32x4*)(lds + K1_TAB + lane * 16) = ((__builtin_bit_cast(f32x4, c[0]) + __builtin_bit_cast(f32x4, c[1])) + (__builtin_bit_cast(f32x4, c[2]) + __builtin_bit_cast(f32x4, c[3]))) + __builtin_bit_cast(f32x4, c[4]);
        *(LAS u32x4*)(lds + K1_TAB + 1024 + lane * 16) = sv;
        asm volatile("s_barrier" ::: "memory");
        lbar();
        }
        publish(ws, 0, tag, true);
        poll<D1>(ws, 0, w1st, tag);
        asm volatile("s_barrier" ::: "memory");
        asm volatile(SVC_KV :: "{v200}"(sl), "{v[204:207]}"(sent), "{s[40:43]}"(srd(ws + WS_KV + (size_t)head * KHS, 64 * KVT)),
                     "{s[44:47]}"(srd(ws + WS_E1 + (size_t)(nt1 * 3 + mt1) * E1S + (1 - kh) * 24576, has1 ? 24576 : 0)), "{s56}"(l0) : "s58", "scc", "memory");
        poll(ws, 0, hw, tag);
        WoTile wt;
        asm volatile(SVC_EARLY : "={v[0:31]}"(wt.w0), "={v[32:63]}"(wt.w1), "={v[64:71]}"(wt.w2), "={v[72:103]}"(wt.x0), "={v[104:107]}"(wt.x1)
                     : "{v200}"(sl), "{v[204:207]}"(sent), "{v[208:215]}"(xo), "{v216}"(xo8), "{s[40:43]}"(srd(ws + WS_WO + (size_t)(cq * 9 + 8) * 18432, 18432)),
                       "{s[44:47]}"(srd(x + (size_t)rg * 32 * 1152 + cq * 288, 31 * 1152 + 288)), "{s[48:51]}"(srd(ws + WS_E2 + (size_t)tl * E2S, has2 ? 196608 : 0)),
                       "{s60}"(j5 * 39936) : "s58", "scc", "memory");
        touch1(code, vc, (j & 7) * 8192, l0 + L_JUNK);
        __builtin_amdgcn_s_setprio(3);
        lbar();
#pragma unroll
        for (int i = 0; i < 9; ++i) *(LAS u32x4*)(lds + L_X + i * 1024 + sl) = i < 8 ? u32x4{wt.x0[4 * i], wt.x0[4 * i + 1], wt.x0[4 * i + 2], wt.x0[4 * i + 3]} : wt.x1;
        publish(ws, 1, tag, true);
        p3_service(wt, ws, lds, w2nd, rg, cq, lane, tag);
        publish(ws, 2, tag, true);
        __builtin_amdgcn_s_setprio(0);
        if (has1) {
            poll<D3>(ws, 2, w3rd, tag);
#pragma unroll 1
            for (int t = 0; t < 16; ++t) { asm volatile("s_barrier" ::: "memory"); if (t == 4) poll(ws, 1, lane == 0 ? b ^ 4 : -1, tag);
                if (t == 11) touch1(code, vc, (j & 7) * 8192, l0 + L_JUNK);
            }
            publish(ws, 3, tag, true);
        }
        if (has2) {
            if (!has1) { __builtin_amdgcn_s_sleep(127); __builtin_amdgcn_s_sleep(127); __builtin_amdgcn_s_sleep(64); }
            poll<D4>(ws, 3, w4th, tag);
            asm volatile("s_barrier" ::: "memory");
            if (b == 0 && lane == 0) st64(rsrc(ws + WS_EP, 8), 0, 0, u32x2{(unsigned)tag, (unsigned)(tag >> 32)});
#pragma unroll 1
            for (int t = 0; t < 13; ++t) { asm volatile("s_barrier" ::: "memory");
                if (t == 4) { const int w = lane * 48 + tl; poll(ws, 1, lane < 5 ? ((w % 30) << 3) | (w / 30) : -1, tag); } }
        }
    } else {
        if (j >= 24) { const u32x2 ep = ld64(rsrc(ws + WS_EP, 8), 0, 0);
            p1_tables(x, wqkv, ws, lds, b - 192, lane, wv); publish(ws, 4, (ep[0] | (u64)ep[1] << 32) + 1, wv == 0);
            p1_wo(wo, ws, b - 192, lane, wv); }
        else { const int rt = (xc >> 1) * 4 + j / 6, cg = (xc & 1) * 6 + j % 6;
            if (cg >= 8) p1_body<true>(x, wqkv, ws, lds, rt, cg, lane, wv); else p1_body<false>(x, wqkv, ws, lds, rt, cg, lane, wv); }
        publish(ws, 0, 0, false);
        int j2 = j; asm volatile("" : "+s"(j2));
        const bool has1 = j2 < 27, has2 = j2 < 30;
        const int kh = xc >> 2, nt1 = (xc & 3) * 9 + j2 / 3, mt1 = j2 % 3;
        const int wl = xc * 30 + j2, j5 = wl / 48, tl = wl - j5 * 48, nt2 = tl / 3, mt2 = tl - nt2 * 3, k0 = 29 * j5, nk2 = j5 < 4 ? 29 : 28;
        const int head = xc * 4 + (j2 >> 3), qb = j2 & 7;
        const u32x4 sw1 = srd(w1 + (size_t)kh * 1024 * 9216, 1024 * 9216), sw2 = srd(w2 + (size_t)k0 * 32 * 4096, has2 ? nk2 * 32 * 4096 : 0);
        u32x4 pw1 = sw1, pw2 = sw2; int pn1 = nt1, pn2 = nt2; asm volatile("" : "+s"(pw1), "+s"(pw2), "+s"(pn1), "+s"(pn2));
        p2_compute(ws, lds, head, qb, fresh_lane(), wv, w1, w2);
        publish(ws, 1, 0, false);
        p3_compute(ws, lds, xc * 8 + (j >> 2), j & 3, fresh_lane(), wv, has1 ? pw1 : pw2, has1 ? 9216 : 4096, has1 ? pn1 : pn2, has1 ? 0 : 3);
        publish(ws, 2, 0, false);
        if (has1) { p4_compute(b1, ws, l0, kh, pn1, mt1, fresh_lane(), wv, pw1, pw2, pn2); publish(ws, 3, 0, false); }
        if (has2) p5_compute(b2, ws, out, l0, j5, tl, k0, nk2, fresh_lane(), wv, pw2);
    }
    asm volatile("Lkernel_end:");
}

extern "C" void kernel_launch(void* const* d_in, const int* in_sizes, int n_in, void* d_out, int out_size, void* d_ws, size_t ws_size, hipStream_t stream) {
    (void)hipFuncSetAttribute((const void*)fused_block, hipFuncAttributeMaxDynamicSharedMemorySize, LDS_BYTES);
    fused_block<<<256, 320, LDS_BYTES, stream>>>((const char*)d_in[0], (const char*)d_in[1], (const unsigned short*)d_in[2], (const char*)d_in[4], (const char*)d_in[5],
                                                 (const char*)d_in[6], (const char*)d_in[7], (char*)d_ws, (char*)d_out);
}
```
